```python
import torch

def _softmax(x, axis=-1):
    return torch.softmax(x, dim=axis)
def _gelu(x):
    return torch.nn.functional.gelu(x, approximate="tanh")
def _silu(x):
    return torch.nn.functional.silu(x)
def _sigmoid(x):
    return torch.sigmoid(x)
def _layernorm(x, axis=-1, eps=1e-5):
    m = x.mean(dim=axis, keepdim=True)
    v = x.var(dim=axis, keepdim=True, unbiased=False)
    return (x - m) / torch.sqrt(v + eps)
def _scatter_add(tgt, idx, src, axis=0):
    return tgt.clone().index_add_(axis, idx, src)
def _segment_sum(src, seg, n):
    out = torch.zeros((n,) + src.shape[1:], dtype=src.dtype)
    return out.index_add_(0, seg, src)
def _pad(x, pw):
    flat = tuple(v for pair in reversed(pw) for v in pair)
    return torch.nn.functional.pad(x, flat)

kernel_name = 'parallel_sum__sliding_local_dilated__glu_gated_swiglu__prompt'

def setup_inputs():

    _w = (lambda a: (lambda t: t.to(torch.bfloat16) if t.is_floating_point() else t)(torch.as_tensor(a)))
    def _A_setup():
        B, S, D = 1, 2048, 640
        return {
            "x": _w((lambda *s: torch.randn(*s, dtype=torch.bfloat16) * 0.05)(B, S, D)),
            "wqkv": _w((lambda *s: torch.randn(*s, dtype=torch.bfloat16) * 0.05)(D, 3 * D)),
            "wo": _w((lambda *s: torch.randn(*s, dtype=torch.bfloat16) * 0.05)(D, D)),
        }
    _A_in = _A_setup()

    def _B_setup():
        B, S, D, F = 1, 2048, 640, 1536
        return {
            "x": _w((lambda *s: torch.randn(*s, dtype=torch.bfloat16) * 0.05)(B, S, D)),
            "w1": _w((lambda *s: torch.randn(*s, dtype=torch.bfloat16) * 0.8)(D, F) * (2.0 / D) ** 0.5),
            "w3": _w((lambda *s: torch.randn(*s, dtype=torch.bfloat16) * 0.8)(D, F) * (2.0 / D) ** 0.5),
            "w2": _w((lambda *s: torch.randn(*s, dtype=torch.bfloat16) * 0.8)(F, D) * (2.0 / F) ** 0.5),
        }
    _B_in = _B_setup()

    return {
        "A_x": _A_in["x"],
        "A_wqkv": _A_in["wqkv"],
        "A_wo": _A_in["wo"],
        "B_x": _B_in["x"],
        "B_w1": _B_in["w1"],
        "B_w3": _B_in["w3"],
        "B_w2": _B_in["w2"],
    }

def reference(A_x, A_wqkv, A_wo, B_x, B_w1, B_w3, B_w2):
    def _A_ref(x, wqkv, wo):
        B, S, D = x.shape
        H = 32; Dh = D // H
        W, dil = 8, 2
        qkv = (lambda x, *d: x.permute(*d))((x @ wqkv).reshape(B, S, 3, H, Dh), 2, 0, 3, 1, 4)
        q, k, v = qkv[0], qkv[1], qkv[2]
        scale = Dh ** -0.5
        pos = torch.arange(S)
        off = torch.arange(W) * dil
        idx = pos[:, None] - off[None, :]
        mask = idx >= 0
        idx_c = torch.where(mask, idx, 0).reshape(-1)
        kg = k[:, :, idx_c, :].reshape(B, H, S, W, Dh)
        vg = v[:, :, idx_c, :].reshape(B, H, S, W, Dh)
        logits = torch.einsum("bhsd,bhswd->bhsw", q, kg) * scale
        logits = torch.where(mask[None, None, :, :], logits, -1e9)
        attn = _softmax(logits, axis=-1)
        o = torch.einsum("bhsw,bhswd->bhsd", attn, vg)
        return (lambda x, *d: x.permute(*d))(o, 0, 2, 1, 3).reshape(B, S, D) @ wo

    def _B_ref(x, w1, w3, w2):
        gate = torch.einsum("bsd,df->bsf", x, w1)
        up = torch.einsum("bsd,df->bsf", x, w3)
        h = _silu(gate) * up
        out = torch.einsum("bsf,fd->bsd", h, w2)
        return out

    x = A_x
    xn = _layernorm(x, axis=-1)
    return x + _A_ref(xn, A_wqkv, A_wo) + _B_ref(xn, B_w1, B_w3, B_w2)
```

```cpp
#include <hip/hip_runtime.h>
#include <hip/hip_bf16.h>
#include <stdint.h>

typedef __attribute__((ext_vector_type(8))) short bf16x8;
typedef __attribute__((ext_vector_type(4))) short bf16x4;
typedef __attribute__((ext_vector_type(4))) float f32x4;
typedef __attribute__((ext_vector_type(2))) __bf16 bf2;
typedef __attribute__((ext_vector_type(2))) unsigned u32x2;
typedef __attribute__((ext_vector_type(4))) unsigned u32x4;

#define LDS_AS __attribute__((address_space(3)))

constexpr int S_ = 2048, D_ = 640, F_ = 1536;
constexpr int KOH = D_ + F_;

__device__ __forceinline__ float rbf(float f) { return (float)(__bf16)f; }
__device__ __forceinline__ unsigned pack2(float lo, float hi) {
  bf2 v; v[0] = (__bf16)lo; v[1] = (__bf16)hi;
  return __builtin_bit_cast(unsigned, v);
}
__device__ __forceinline__ float bflo(unsigned u) { return __builtin_bit_cast(float, u << 16); }
__device__ __forceinline__ float bfhi(unsigned u) { return __builtin_bit_cast(float, u & 0xffff0000u); }

__device__ __forceinline__ float dot2(unsigned a, unsigned b, float c) {
  return __builtin_amdgcn_fdot2_f32_bf16(__builtin_bit_cast(bf2, a), __builtin_bit_cast(bf2, b), c, false);
}
template <int N> __device__ __forceinline__ void wait_vm() {
  asm volatile("s_waitcnt vmcnt(%0)" ::"i"(N) : "memory");
}
__device__ __forceinline__ void glds16(const void* g, char* l) {
  __builtin_amdgcn_global_load_lds((const unsigned*)g, (unsigned*)l, 16, 0, 0);
}
template <int IMM>
__device__ __forceinline__ void tr_issue(bf16x4& d, unsigned addr) {
  asm volatile("ds_read_b64_tr_b16 %0, %1 offset:%2" : "=v"(d) : "v"(addr), "i"(IMM) : "memory");
}
template <int IMM>
__device__ __forceinline__ void rd128_issue(bf16x8& d, unsigned addr) {
  asm volatile("ds_read_b128 %0, %1 offset:%2" : "=v"(d) : "v"(addr), "i"(IMM) : "memory");
}
__device__ __forceinline__ void pin(bf16x8 (&a)[4]) {
  asm volatile("" : "+v"(a[0]), "+v"(a[1]), "+v"(a[2]), "+v"(a[3]));
}
__device__ __forceinline__ void pin(bf16x8 (&a)[5]) {
  asm volatile("" : "+v"(a[0]), "+v"(a[1]), "+v"(a[2]), "+v"(a[3]), "+v"(a[4]));
}
__device__ __forceinline__ void lgkm_pin(bf16x4 (&l)[4], bf16x4 (&h)[4]) {
  asm volatile("s_waitcnt lgkmcnt(0)"
               : "+v"(l[0]), "+v"(l[1]), "+v"(l[2]), "+v"(l[3]), "+v"(h[0]), "+v"(h[1]), "+v"(h[2]), "+v"(h[3])
               :: "memory");
}
__device__ __forceinline__ void lgkm_pin(bf16x4 (&l)[5], bf16x4 (&h)[5]) {
  asm volatile("s_waitcnt lgkmcnt(0)"
               : "+v"(l[0]), "+v"(l[1]), "+v"(l[2]), "+v"(l[3]), "+v"(l[4]), "+v"(h[0]), "+v"(h[1]), "+v"(h[2]),
                 "+v"(h[3]), "+v"(h[4])
               :: "memory");
}
__device__ __forceinline__ void lgkm_pin(bf16x4 (&l)[6], bf16x4 (&h)[6]) {
  asm volatile("s_waitcnt lgkmcnt(0)"
               : "+v"(l[0]), "+v"(l[1]), "+v"(l[2]), "+v"(l[3]), "+v"(l[4]), "+v"(l[5]), "+v"(h[0]), "+v"(h[1]),
                 "+v"(h[2]), "+v"(h[3]), "+v"(h[4]), "+v"(h[5])
               :: "memory");
}
__device__ __forceinline__ bf16x8 cat8(bf16x4 a, bf16x4 b) {
  return __builtin_shufflevector(a, b, 0, 1, 2, 3, 4, 5, 6, 7);
}

__global__ __launch_bounds__(256) void k_ln(const unsigned* __restrict__ x, unsigned* __restrict__ xn) {
  const int lane = threadIdx.x & 63;
  const int row = blockIdx.x * 4 + (threadIdx.x >> 6);
  const unsigned* xr = x + (size_t)row * (D_ / 2);
  unsigned v[5];
#pragma unroll
  for (int i = 0; i < 5; ++i) v[i] = xr[lane + 64 * i];
  float a[10];
  float s = 0.f;
#pragma unroll
  for (int i = 0; i < 5; ++i) {
    a[2 * i] = bflo(v[i]); a[2 * i + 1] = bfhi(v[i]);
    s += a[2 * i] + a[2 * i + 1];
  }
#pragma unroll
  for (int o = 32; o > 0; o >>= 1) s += __shfl_xor(s, o);
  const float mean = s * (1.0f / D_);
  float q = 0.f;
#pragma unroll
  for (int i = 0; i < 10; ++i) { float d = a[i] - mean; q += d * d; }
#pragma unroll
  for (int o = 32; o > 0; o >>= 1) q += __shfl_xor(q, o);
  const float var = q * (1.0f / D_);
  const float mb = rbf(mean);
  const float sd = rbf(sqrtf(rbf(rbf(var) + 1e-5f)));
  unsigned* o = xn + (size_t)row * (D_ / 2);
#pragma unroll
  for (int i = 0; i < 5; ++i) {
    float l = rbf(a[2 * i] - mb) / sd;
    float h = rbf(a[2 * i + 1] - mb) / sd;
    o[lane + 64 * i] = pack2(l, h);
  }
}

constexpr int G1_LDS = 147456;

struct GuCtx {
  const char* a_src;
  const char* b_src[3];
  int a_rd;
  int b_rd[6];
  int wid;
};

template <int T>
__device__ __forceinline__ void gu_issue(char* smem, const GuCtx& c) {
  constexpr int ST = (T + 4) / 2;
  constexpr int HF = (T + 4) & 1;
  if constexpr (ST < 10) {
    glds16(c.a_src + (size_t)HF * 64 * D_ * 2 + ST * 128, smem + (ST % 3) * 16384 + (HF * 64 + c.wid * 8) * 128);
  }
  constexpr int BT = T + 3;
  if constexpr (BT >= 0 && BT < 20) {
#pragma unroll
    for (int j = 0; j < 3; ++j)
      glds16(c.b_src[j] + (size_t)BT * 32 * F_ * 2, smem + 49152 + (BT & 3) * 24576 + (c.wid * 3 + j) * 1024);
  }
}
template <int T> constexpr int gu_gsize() {
  return (T < -4) ? 0 : (((T + 4) / 2 < 10) ? 1 : 0) + ((T + 3 >= 0 && T + 3 < 20) ? 3 : 0);
}

struct GuFrag { bf16x8 a[4]; bf16x4 bl[6], bh[6]; };

__device__ __forceinline__ void gu_mma(GuFrag& f, f32x4 (&acc)[4][6]) {
#pragma unroll
  for (int m = 0; m < 4; ++m)
#pragma unroll
    for (int n = 0; n < 6; ++n)
      acc[m][n] = __builtin_amdgcn_mfma_f32_16x16x32_bf16(cat8(f.bl[n], f.bh[n]), f.a[m], acc[m][n], 0, 0, 0);
}

template <int T>
__device__ __forceinline__ void gu_step(char* smem, unsigned lbase, const GuCtx& c, f32x4 (&acc)[4][6],
                                        GuFrag& prev, GuFrag& cur) {
  __builtin_amdgcn_sched_barrier(0);
  if constexpr (T > 0) { lgkm_pin(prev.bl, prev.bh); pin(prev.a); }
  if constexpr (T < 20) {
    wait_vm<gu_gsize<T - 2>() + gu_gsize<T - 1>()>();
    __builtin_amdgcn_s_barrier();
    gu_issue<T>(smem, c);
    const unsigned ab = lbase + ((T & 1) ? (c.a_rd ^ 64) : c.a_rd);
    rd128_issue<((T / 2) % 3) * 16384>(cur.a[0], ab);
    rd128_issue<((T / 2) % 3) * 16384 + 2048>(cur.a[1], ab);
    rd128_issue<((T / 2) % 3) * 16384 + 4096>(cur.a[2], ab);
    rd128_issue<((T / 2) % 3) * 16384 + 6144>(cur.a[3], ab);
    const unsigned bb = lbase + 49152 + ((T & 3) >> 1) * 49152;
#pragma unroll
    for (int n = 0; n < 6; ++n) {
      tr_issue<(T & 1) * 24576>(cur.bl[n], bb + c.b_rd[n]);
      tr_issue<(T & 1) * 24576 + 3072>(cur.bh[n], bb + c.b_rd[n]);
    }
    __builtin_amdgcn_sched_barrier(0);
  }
  if constexpr (T > 0) gu_mma(prev, acc);
  if constexpr (T < 20) gu_step<T + 1>(smem, lbase, c, acc, cur, prev);
}

__device__ __forceinline__ void gu_block(char* smem, int rt, int slice, const char* xn, const char* w1,
                                         const char* w3, char* oh) {
  const int tid = threadIdx.x, lane = tid & 63;
  const int wid = __builtin_amdgcn_readfirstlane(tid >> 6);
  const int wm = wid >> 2, wn = wid & 3;
  const int row0 = rt * 128, f0 = slice * 192;
  GuCtx c;
  c.wid = wid;
  {
    const int r = lane >> 3, ch = (lane & 7) ^ r;
    c.a_src = xn + ((size_t)(row0 + wid * 8 + r) * D_ + ch * 8) * 2;
  }
#pragma unroll
  for (int j = 0; j < 3; ++j) {
    const int off = (wid * 3 + j) * 1024 + lane * 16;
    const int k = off / 768, rem = off % 768;
    const int seg = rem >> 8, up = (rem >> 5) & 7, h = (rem >> 4) & 1;
    const int f = (k & 3) | (((k >> 3) & 1) << 2);
    const int col = (seg * 8 + (up ^ f)) * 16 + h * 8;
    const char* w = col < 192 ? w1 : w3;
    const int cc = col < 192 ? col : col - 192;
    c.b_src[j] = w + ((size_t)k * F_ + f0 + cc) * 2;
  }
  const int g = lane >> 4, q = (lane >> 2) & 3, p = lane & 3, fr = lane & 15;
  c.a_rd = (wm * 64 + fr) * 128 + ((g ^ (lane & 7)) << 4);
#pragma unroll
  for (int n = 0; n < 6; ++n) {
    const int U = (n < 3) ? 3 * wn + n : 12 + 3 * wn + (n - 3);
    const int f = q | ((g & 1) << 2);
    c.b_rd[n] = (8 * g + q) * 768 + (U >> 3) * 256 + (((U & 7) ^ f) << 5) + p * 8;
  }
  f32x4 acc[4][6];
#pragma unroll
  for (int m = 0; m < 4; ++m)
#pragma unroll
    for (int n = 0; n < 6; ++n) acc[m][n] = f32x4{0.f, 0.f, 0.f, 0.f};

  gu_issue<-4>(smem, c);
  gu_issue<-3>(smem, c);
  gu_issue<-2>(smem, c);
  gu_issue<-1>(smem, c);
  {
    GuFrag f0_, f1_;
    gu_step<0>(smem, (unsigned)(size_t)smem, c, acc, f0_, f1_);
  }

#pragma unroll
  for (int m = 0; m < 4; ++m) {
    const int row = row0 + wm * 64 + m * 16 + fr;
#pragma unroll
    for (int n = 0; n < 3; ++n) {
      float hv[4];
#pragma unroll
      for (int r = 0; r < 4; ++r) {
        const float gt = rbf(acc[m][n][r]);
        const float up = rbf(acc[m][n + 3][r]);
        const float sl = rbf(gt / (1.0f + __expf(-gt)));
        hv[r] = sl * up;
      }
      u32x2 o;
      o[0] = pack2(hv[0], hv[1]);
      o[1] = pack2(hv[2], hv[3]);
      *(u32x2*)(oh + ((size_t)row * KOH + D_ + f0 + wn * 48 + n * 16 + g * 4) * 2) = o;
    }
  }
}

struct QkvCtx {
  const char* a_src;
  const char* ah_src;
  const char* ah_src1;
  const char* b_src[2];
  int a_rd;
  int b_rd[4];
  int wid;
  int lane;
};
constexpr int QA_STAGE = 144 * 128;
constexpr int QB_BASE = 3 * QA_STAGE;
constexpr int QB_SLOT = 32 * 512;

template <int T>
__device__ __forceinline__ void qkv_issue(char* smem, const QkvCtx& c) {
  constexpr int ST = (T + 4) / 2;
  constexpr int HF = (T + 4) & 1;
  if constexpr (ST < 10) {
    glds16(c.a_src + (size_t)HF * 64 * D_ * 2 + ST * 128,
           smem + (ST % 3) * QA_STAGE + (16 + HF * 64 + c.wid * 8) * 128);
    if (c.lane < 8)
      glds16((HF ? c.ah_src1 : c.ah_src) + ST * 128, smem + (ST % 3) * QA_STAGE + (HF * 8 + c.wid) * 128);
  }
  constexpr int BT = T + 3;
  if constexpr (BT >= 0 && BT < 20) {
#pragma unroll
    for (int j = 0; j < 2; ++j)
      glds16(c.b_src[j] + (size_t)BT * 32 * (3 * D_) * 2, smem + QB_BASE + (BT & 3) * QB_SLOT + (c.wid * 2 + j) * 1024);
  }
}
template <int T> constexpr int qkv_gsize() {
  return (T < -4) ? 0 : (((T + 4) / 2 < 10) ? 2 : 0) + ((T + 3 >= 0 && T + 3 < 20) ? 2 : 0);
}

template <int MT> struct QkvFrag { bf16x8 a[MT]; bf16x4 bl[4], bh[4]; };

template <int T, int MT>
__device__ __forceinline__ void qkv_step(char* smem, unsigned lbase, const QkvCtx& c, f32x4 (&acc)[MT][4],
                                         QkvFrag<MT>& prev, QkvFrag<MT>& cur) {
  __builtin_amdgcn_sched_barrier(0);
  if constexpr (T > 0) { lgkm_pin(prev.bl, prev.bh); pin(prev.a); }
  if constexpr (T < 20) {
    wait_vm<qkv_gsize<T - 2>() + qkv_gsize<T - 1>()>();
    __builtin_amdgcn_s_barrier();
    qkv_issue<T>(smem, c);
    const unsigned ab = lbase + ((T & 1) ? (c.a_rd ^ 64) : c.a_rd);
    rd128_issue<((T / 2) % 3) * QA_STAGE>(cur.a[0], ab);
    rd128_issue<((T / 2) % 3) * QA_STAGE + 2048>(cur.a[1], ab);
    rd128_issue<((T / 2) % 3) * QA_STAGE + 4096>(cur.a[2], ab);
    rd128_issue<((T / 2) % 3) * QA_STAGE + 6144>(cur.a[3], ab);
    if constexpr (MT > 4) rd128_issue<((T / 2) % 3) * QA_STAGE + 8192>(cur.a[MT - 1], ab);
    const unsigned bb = lbase + QB_BASE + ((T & 3) >> 1) * 2 * QB_SLOT;
#pragma unroll
    for (int n = 0; n < 4; ++n) {
      tr_issue<(T & 1) * QB_SLOT>(cur.bl[n], bb + c.b_rd[n]);
      tr_issue<(T & 1) * QB_SLOT + 2048>(cur.bh[n], bb + c.b_rd[n]);
    }
    __builtin_amdgcn_sched_barrier(0);
  }
  if constexpr (T > 0) {
#pragma unroll
    for (int m = 0; m < MT; ++m)
#pragma unroll
      for (int n = 0; n < 4; ++n)
        acc[m][n] = __builtin_amdgcn_mfma_f32_16x16x32_bf16(cat8(prev.bl[n], prev.bh[n]), prev.a[m], acc[m][n], 0, 0, 0);
  }
  if constexpr (T < 20) qkv_step<T + 1, MT>(smem, lbase, c, acc, cur, prev);
}

constexpr int QE_STRIDE = 488;

template <int MT>
__device__ __forceinline__ void qkv_main(char* smem, const QkvCtx& c, int arow0, int wn, bool negrows) {
  f32x4 acc[MT][4];
#pragma unroll
  for (int m = 0; m < MT; ++m)
#pragma unroll
    for (int n = 0; n < 4; ++n) acc[m][n] = f32x4{0.f, 0.f, 0.f, 0.f};
  {
    QkvFrag<MT> f0_, f1_;
    qkv_step<0, MT>(smem, (unsigned)(size_t)smem, c, acc, f0_, f1_);
  }
  __builtin_amdgcn_s_barrier();
  const int g = c.lane >> 4, fr = c.lane & 15;
#pragma unroll
  for (int m = 0; m < MT; ++m) {
    const int ar = arow0 + m * 16 + fr;
#pragma unroll
    for (int n = 0; n < 4; ++n) {
      const int U = wn * 4 + n;
      if (U < 15) {
        u32x2 o;
        o[0] = pack2(acc[m][n][0], acc[m][n][1]);
        o[1] = pack2(acc[m][n][2], acc[m][n][3]);
        *(u32x2*)(smem + ar * QE_STRIDE + U * 32 + g * 8) = o;
      }
    }
  }
}

__device__ __forceinline__ void qkv_block(char* smem, int rt, int hg, const char* xn, const char* wqkv, char* oh) {
  const int tid = threadIdx.x, lane = tid & 63;
  const int wid = __builtin_amdgcn_readfirstlane(tid >> 6);
  const int wm = wid >> 2, wn = wid & 3;
  const int s0 = rt * 128;
  QkvCtx c;
  c.wid = wid;
  c.lane = lane;
  {
    const int r = lane >> 3, ch = (lane & 7) ^ r;
    c.a_src = xn + ((size_t)(s0 + wid * 8 + r) * D_ + ch * 8) * 2;
    const int chh = (lane & 7) ^ wid;
    int r0 = s0 - 16 + wid, r1 = s0 - 8 + wid;
    r0 = r0 < 0 ? 0 : r0;
    r1 = r1 < 0 ? 0 : r1;
    c.ah_src = xn + ((size_t)r0 * D_ + chh * 8) * 2;
    c.ah_src1 = xn + ((size_t)r1 * D_ + chh * 8) * 2;
  }
#pragma unroll
  for (int j = 0; j < 2; ++j) {
    const int off = (wid * 2 + j) * 1024 + lane * 16;
    const int k = off >> 9, rem = off & 511;
    const int seg = rem >> 8, up = (rem >> 5) & 7, h = (rem >> 4) & 1;
    const int f = (k & 3) | (((k >> 3) & 1) << 2);
    int U = seg * 8 + (up ^ f);
    U = U > 14 ? 14 : U;
    const int sec = U / 5, uu = U % 5;
    c.b_src[j] = wqkv + ((size_t)k * (3 * D_) + sec * D_ + hg * 80 + uu * 16 + h * 8) * 2;
  }
  const int g = lane >> 4, q = (lane >> 2) & 3, p = lane & 3, fr = lane & 15;
  const int arow0 = wm ? 80 : 0;
  c.a_rd = (arow0 + fr) * 128 + ((g ^ (lane & 7)) << 4);
#pragma unroll
  for (int n = 0; n < 4; ++n) {
    const int U = wn * 4 + n;
    const int f = q | ((g & 1) << 2);
    c.b_rd[n] = (8 * g + q) * 512 + (U >> 3) * 256 + (((U & 7) ^ f) << 5) + p * 8;
  }
  qkv_issue<-4>(smem, c);
  qkv_issue<-3>(smem, c);
  qkv_issue<-2>(smem, c);
  qkv_issue<-1>(smem, c);
  if (wm == 0) qkv_main<5>(smem, c, arow0, wn, false);
  else qkv_main<4>(smem, c, arow0, wn, false);
  __syncthreads();

  const int hh = tid >> 7, r = tid & 127;
  const int s = s0 + r;
  const char* qp = smem + (16 + r) * QE_STRIDE + hh * 40;
  unsigned qv[10];
#pragma unroll
  for (int i = 0; i < 5; ++i) {
    u32x2 t = *(const u32x2*)(qp + i * 8);
    qv[2 * i] = t[0]; qv[2 * i + 1] = t[1];
  }
  float lg[8];
  float mx = -3.0e38f;
#pragma unroll
  for (int w = 0; w < 8; ++w) {
    const char* kp = qp + 160 - 2 * w * QE_STRIDE;
    float d = 0.f;
#pragma unroll
    for (int i = 0; i < 5; ++i) {
      u32x2 t = *(const u32x2*)(kp + i * 8);
      d = dot2(qv[2 * i], t[0], d);
      d = dot2(qv[2 * i + 1], t[1], d);
    }
    float l = rbf(rbf(d) * 0.22360679774997896f);
    l = (s - 2 * w >= 0) ? l : rbf(-1e9f);
    lg[w] = l;
    mx = fmaxf(mx, l);
  }
  float sum = 0.f;
#pragma unroll
  for (int w = 0; w < 8; ++w) { lg[w] = __expf(lg[w] - mx); sum += lg[w]; }
  const float inv = 1.0f / sum;
  float o[20];
#pragma unroll
  for (int i = 0; i < 20; ++i) o[i] = 0.f;
#pragma unroll
  for (int w = 0; w < 8; ++w) {
    const float pw = rbf(lg[w] * inv);
    const char* vp = qp + 320 - 2 * w * QE_STRIDE;
#pragma unroll
    for (int i = 0; i < 5; ++i) {
      u32x2 t = *(const u32x2*)(vp + i * 8);
      o[4 * i + 0] += pw * bflo(t[0]);
      o[4 * i + 1] += pw * bfhi(t[0]);
      o[4 * i + 2] += pw * bflo(t[1]);
      o[4 * i + 3] += pw * bfhi(t[1]);
    }
  }
#pragma unroll
  for (int i = 0; i < 5; ++i) {
    u32x2 t;
    t[0] = pack2(o[4 * i], o[4 * i + 1]);
    t[1] = pack2(o[4 * i + 2], o[4 * i + 3]);
    *(u32x2*)(smem + (16 + r) * QE_STRIDE + hh * 40 + i * 8) = t;
  }
  __syncthreads();
#pragma unroll
  for (int i = 0; i < 5; ++i) {
    const int idx = tid + 512 * i;
    const int rr = idx / 20, part = idx % 20;
    u32x2 t = *(const u32x2*)(smem + (16 + rr) * QE_STRIDE + part * 8);
    *(u32x2*)(oh + ((size_t)(s0 + rr) * KOH + hg * 80) * 2 + part * 8) = t;
  }
}

__global__ __launch_bounds__(512, 2) void k_gemm1(const char* __restrict__ xn, const char* __restrict__ wqkv,
                                                  const char* __restrict__ w1, const char* __restrict__ w3,
                                                  char* __restrict__ oh) {
  __shared__ __attribute__((aligned(16))) char smem[G1_LDS];
  const int b = blockIdx.x;
  const int x = b & 7, idx = b >> 3;
  const int rh = x & 1, j = x >> 1;
  const int i2 = idx & 15;
  const int rt = rh * 8 + (i2 >> 1), cs = 2 * j + (i2 & 1);
  if (idx < 16) gu_block(smem, rt, cs, xn, w1, w3, oh);
  else qkv_block(smem, rt, cs, xn, wqkv, oh);
}

constexpr int G2_WBUF = 8192 + 10240;
constexpr int G2_LDS = 4 * 2 * G2_WBUF;

__global__ __launch_bounds__(256, 1) void k_gemm2(const char* __restrict__ oh, const char* __restrict__ wo,
                                                  const char* __restrict__ w2, const char* __restrict__ xin,
                                                  char* __restrict__ out) {
  __shared__ __attribute__((aligned(16))) char smem[G2_LDS];
  const int tid = threadIdx.x, lane = tid & 63;
  const int wid = __builtin_amdgcn_readfirstlane(tid >> 6);
  const int b = blockIdx.x;
  const int x = b & 7, idx = b >> 3;
  const int rt = (x >> 1) * 8 + (idx >> 2), ct = (x & 1) * 4 + (idx & 3);
  const int row0 = rt * 64, col0 = ct * 80;
  char* wbuf = smem + wid * 2 * G2_WBUF;

  const char* a_src;
  {
    const int r = lane >> 3, ch = (lane & 7) ^ r;
    a_src = oh + ((size_t)(row0 + r) * KOH + ch * 8) * 2;
  }
  int b_off[10];
#pragma unroll
  for (int pz = 0; pz < 10; ++pz) {
    const int off = pz * 1024 + lane * 16;
    const int rho = off / 160, chunk = (off % 160) >> 4;
    const int half = rho >> 5, a = (rho >> 4) & 1, jh = (rho >> 3) & 1, bb = (rho >> 2) & 1, q = rho & 3;
    const int k = 32 * half + 8 * (2 * a + bb) + 4 * jh + q;
    b_off[pz] = (k * D_ + col0 + chunk * 8) * 2;
  }
  const int g = lane >> 4, q = (lane >> 2) & 3, p = lane & 3, fr = lane & 15;
  const int a_rd = fr * 128 + ((g ^ (lane & 7)) << 4);
  const int b_rd = 8192 + (16 * (g >> 1) + 4 * (g & 1) + q) * 160 + p * 8;

  auto issue = [&](int c, int buf) {
    char* dst = wbuf + buf * G2_WBUF;
    const char* as = a_src + c * 128;
#pragma unroll
    for (int pz = 0; pz < 8; ++pz) glds16(as + (size_t)pz * 8 * KOH * 2, dst + pz * 1024);
    const char* wb = (c < 10) ? wo + (size_t)c * 64 * D_ * 2 : w2 + (size_t)(c - 10) * 64 * D_ * 2;
#pragma unroll
    for (int pz = 0; pz < 10; ++pz) glds16(wb + b_off[pz], dst + 8192 + pz * 1024);
  };

  f32x4 acc[4][5], accA[4][5];
#pragma unroll
  for (int m = 0; m < 4; ++m)
#pragma unroll
    for (int n = 0; n < 5; ++n) { acc[m][n] = f32x4{0.f, 0.f, 0.f, 0.f}; accA[m][n] = acc[m][n]; }

  issue(wid, 0);
#pragma unroll 1
  for (int i = 0; i < 9; ++i) {
    const int c = wid + 4 * i;
    if (c >= 34) break;
    if (c + 4 < 34) { issue(c + 4, (i + 1) & 1); wait_vm<18>(); }
    else wait_vm<0>();
    const char* base = wbuf + (i & 1) * G2_WBUF;
#pragma unroll
    for (int kk = 0; kk < 2; ++kk) {
      bf16x8 a[4];
      bf16x4 bl[5], bh[5];
#pragma unroll
      for (int m = 0; m < 4; ++m) a[m] = *(const bf16x8*)(base + (kk ? (a_rd ^ 64) : a_rd) + m * 2048);
      const unsigned ba = (unsigned)(size_t)base + b_rd + kk * 32 * 160;
      tr_issue<0>(bl[0], ba);   tr_issue<1280>(bh[0], ba);
      tr_issue<32>(bl[1], ba);  tr_issue<1280 + 32>(bh[1], ba);
      tr_issue<64>(bl[2], ba);  tr_issue<1280 + 64>(bh[2], ba);
      tr_issue<96>(bl[3], ba);  tr_issue<1280 + 96>(bh[3], ba);
      tr_issue<128>(bl[4], ba); tr_issue<1280 + 128>(bh[4], ba);
      lgkm_pin(bl, bh);
#pragma unroll
      for (int m = 0; m < 4; ++m)
#pragma unroll
        for (int n = 0; n < 5; ++n)
          acc[m][n] = __builtin_amdgcn_mfma_f32_16x16x32_bf16(cat8(bl[n], bh[n]), a[m], acc[m][n], 0, 0, 0);
    }
    if (c < 10 && c + 4 >= 10) {
#pragma unroll
      for (int m = 0; m < 4; ++m)
#pragma unroll
        for (int n = 0; n < 5; ++n) { accA[m][n] = acc[m][n]; acc[m][n] = f32x4{0.f, 0.f, 0.f, 0.f}; }
    }
  }
  float* red = (float*)smem;
  const int orow = tid >> 2, ocol = (tid & 3) * 20;
  float sA[20], sB[20];
  __syncthreads();
#pragma unroll
  for (int m = 0; m < 4; ++m)
#pragma unroll
    for (int n = 0; n < 5; ++n)
      *(f32x4*)(red + wid * 5120 + (m * 16 + fr) * 80 + n * 16 + g * 4) = accA[m][n];
  __syncthreads();
#pragma unroll
  for (int i = 0; i < 5; ++i) {
    f32x4 t = *(const f32x4*)(red + orow * 80 + ocol + i * 4);
#pragma unroll
    for (int w = 1; w < 4; ++w) t += *(const f32x4*)(red + w * 5120 + orow * 80 + ocol + i * 4);
#pragma unroll
    for (int e = 0; e < 4; ++e) sA[i * 4 + e] = t[e];
  }
  __syncthreads();
#pragma unroll
  for (int m = 0; m < 4; ++m)
#pragma unroll
    for (int n = 0; n < 5; ++n)
      *(f32x4*)(red + wid * 5120 + (m * 16 + fr) * 80 + n * 16 + g * 4) = acc[m][n];
  __syncthreads();
#pragma unroll
  for (int i = 0; i < 5; ++i) {
    f32x4 t = *(const f32x4*)(red + orow * 80 + ocol + i * 4);
#pragma unroll
    for (int w = 1; w < 4; ++w) t += *(const f32x4*)(red + w * 5120 + orow * 80 + ocol + i * 4);
#pragma unroll
    for (int e = 0; e < 4; ++e) sB[i * 4 + e] = t[e];
  }
  const size_t go = ((size_t)(row0 + orow) * D_ + col0 + ocol) * 2;
#pragma unroll
  for (int i = 0; i < 5; ++i) {
    u32x2 xv = *(const u32x2*)(xin + go + i * 8);
    float xs[4] = {bflo(xv[0]), bfhi(xv[0]), bflo(xv[1]), bfhi(xv[1])};
    float r[4];
#pragma unroll
    for (int e = 0; e < 4; ++e) r[e] = rbf(xs[e] + rbf(sA[i * 4 + e])) + rbf(sB[i * 4 + e]);
    u32x2 o;
    o[0] = pack2(r[0], r[1]);
    o[1] = pack2(r[2], r[3]);
    *(u32x2*)(out + go + i * 8) = o;
  }
}

extern "C" void kernel_launch(void* const* d_in, const int* in_sizes, int n_in, void* d_out, int out_size,
                              void* d_ws, size_t ws_size, hipStream_t stream) {
  const char* x = (const char*)d_in[0];
  const char* wqkv = (const char*)d_in[1];
  const char* wo = (const char*)d_in[2];
  const char* w1 = (const char*)d_in[4];
  const char* w3 = (const char*)d_in[5];
  const char* w2 = (const char*)d_in[6];
  char* xn = (char*)d_ws;
  char* oh = (char*)d_ws + (4u << 20);
  k_ln<<<S_ / 4, 256, 0, stream>>>((const unsigned*)x, (unsigned*)xn);
  k_gemm1<<<256, 512, 0, stream>>>(xn, wqkv, w1, w3, oh);
  k_gemm2<<<256, 256, 0, stream>>>(oh, wo, w2, x, (char*)d_out);
}
```

```cpp
#include <hip/hip_runtime.h>
#include <hip/hip_bf16.h>
#include <stdint.h>
#include <type_traits>

typedef __attribute__((ext_vector_type(8))) short bf16x8;
typedef __attribute__((ext_vector_type(4))) short bf16x4;
typedef __attribute__((ext_vector_type(4))) float f32x4;
typedef __attribute__((ext_vector_type(2))) float f32x2;
typedef __attribute__((ext_vector_type(2))) __bf16 bf2;
typedef __attribute__((ext_vector_type(2))) unsigned u32x2;
typedef __attribute__((ext_vector_type(4))) unsigned u32x4;

#define STAMP(base, i)
constexpr int S_ = 2048, D_ = 640, F_ = 1536;
constexpr int KOH = D_ + F_;
constexpr unsigned WS_OH = 4u << 20, WS_P2 = 24u << 20;

__device__ __forceinline__ float rbf(float f) { return (float)(__bf16)f; }
__device__ __forceinline__ unsigned pack2(float lo, float hi) {
  bf2 v; v[0] = (__bf16)lo; v[1] = (__bf16)hi;
  return __builtin_bit_cast(unsigned, v);
}
__device__ __forceinline__ float bflo(unsigned u) { return __builtin_bit_cast(float, u << 16); }
__device__ __forceinline__ float bfhi(unsigned u) { return __builtin_bit_cast(float, u & 0xffff0000u); }
__device__ __forceinline__ f32x2 rbf2(f32x2 v) {
  const unsigned u = pack2(v[0], v[1]);
  return f32x2{bflo(u), bfhi(u)};
}
__device__ __forceinline__ float dot2(unsigned a, unsigned b, float c) {
  return __builtin_amdgcn_fdot2_f32_bf16(__builtin_bit_cast(bf2, a), __builtin_bit_cast(bf2, b), c, false);
}
#define ST_AUX 0
typedef __amdgpu_buffer_rsrc_t rsrc_t;
__device__ __forceinline__ rsrc_t mk_rsrc(void* p, unsigned bytes) {
  return __builtin_amdgcn_make_buffer_rsrc(p, 0, bytes, 0x00020000);
}
__device__ __forceinline__ void st16(rsrc_t r, unsigned off, u32x4 v) {
  __builtin_amdgcn_raw_buffer_store_b128(v, r, off, 0, ST_AUX);
}
__device__ __forceinline__ void st8(rsrc_t r, unsigned off, u32x2 v) {
  __builtin_amdgcn_raw_buffer_store_b64(v, r, off, 0, ST_AUX);
}
template <int N> __device__ __forceinline__ void wait_vm() {
  asm volatile("s_waitcnt vmcnt(%0)" ::"i"(N) : "memory");
}
__device__ __forceinline__ void glds16(const void* g, char* l) {
  __builtin_amdgcn_global_load_lds((const unsigned*)g, (unsigned*)l, 16, 0, 0);
}
__device__ __forceinline__ void glds16_lo(const void* g, unsigned lds_addr, unsigned long long mask) {
  unsigned keep;
  asm volatile(
      "s_mov_b32 %0, m0\n\ts_mov_b32 m0, %2\n\ts_mov_b64 exec, %3\n\tglobal_load_lds_dwordx4 %1, off\n\t"
      "s_mov_b64 exec, -1\n\ts_mov_b32 m0, %0"
      : "=&s"(keep) : "v"(g), "s"(lds_addr), "s"(mask) : "memory");
}
template <int IMM>
__device__ __forceinline__ void tr_issue(bf16x4& d, unsigned addr) {
  asm volatile("ds_read_b64_tr_b16 %0, %1 offset:%2" : "=v"(d) : "v"(addr), "i"(IMM) : "memory");
}
template <int IMM>
__device__ __forceinline__ void rd128_issue(bf16x8& d, unsigned addr) {
  asm volatile("ds_read_b128 %0, %1 offset:%2" : "=v"(d) : "v"(addr), "i"(IMM) : "memory");
}
__device__ __forceinline__ void pin(bf16x8 (&a)[4]) {
  asm volatile("" : "+v"(a[0]), "+v"(a[1]), "+v"(a[2]), "+v"(a[3]));
}
__device__ __forceinline__ void pin(bf16x8 (&a)[5]) {
  asm volatile("" : "+v"(a[0]), "+v"(a[1]), "+v"(a[2]), "+v"(a[3]), "+v"(a[4]));
}
__device__ __forceinline__ void lgkm_pin(bf16x4 (&l)[4], bf16x4 (&h)[4]) {
  asm volatile("s_waitcnt lgkmcnt(0)"
               : "+v"(l[0]), "+v"(l[1]), "+v"(l[2]), "+v"(l[3]), "+v"(h[0]), "+v"(h[1]), "+v"(h[2]), "+v"(h[3])
               :: "memory");
}
__device__ __forceinline__ void lgkm_pin(bf16x4 (&l)[5], bf16x4 (&h)[5]) {
  asm volatile("s_waitcnt lgkmcnt(0)"
               : "+v"(l[0]), "+v"(l[1]), "+v"(l[2]), "+v"(l[3]), "+v"(l[4]), "+v"(h[0]), "+v"(h[1]), "+v"(h[2]),
                 "+v"(h[3]), "+v"(h[4])
               :: "memory");
}
__device__ __forceinline__ void lgkm_pin(bf16x4 (&l)[6], bf16x4 (&h)[6]) {
  asm volatile("s_waitcnt lgkmcnt(0)"
               : "+v"(l[0]), "+v"(l[1]), "+v"(l[2]), "+v"(l[3]), "+v"(l[4]), "+v"(l[5]), "+v"(h[0]), "+v"(h[1]),
                 "+v"(h[2]), "+v"(h[3]), "+v"(h[4]), "+v"(h[5])
               :: "memory");
}
__device__ __forceinline__ bf16x8 cat8(bf16x4 a, bf16x4 b) {
  return __builtin_shufflevector(a, b, 0, 1, 2, 3, 4, 5, 6, 7);
}

__global__ __launch_bounds__(256) void k_ln(const unsigned* __restrict__ x, unsigned* __restrict__ xn) {
  STAMP((char*)xn + (70u << 20), 0);
  const int lane = threadIdx.x & 63;
  const int row = blockIdx.x * 4 + (threadIdx.x >> 6);
  const unsigned* xr = x + (size_t)row * (D_ / 2);
  unsigned v[5];
#pragma unroll
  for (int i = 0; i < 5; ++i) v[i] = xr[lane + 64 * i];
  float a[10];
  float s = 0.f;
#pragma unroll
  for (int i = 0; i < 5; ++i) {
    a[2 * i] = bflo(v[i]); a[2 * i + 1] = bfhi(v[i]);
    s += a[2 * i] + a[2 * i + 1];
  }
#pragma unroll
  for (int o = 32; o > 0; o >>= 1) s += __shfl_xor(s, o);
  const float mean = s * (1.0f / D_);
  float q = 0.f;
#pragma unroll
  for (int i = 0; i < 10; ++i) { float d = a[i] - mean; q += d * d; }
#pragma unroll
  for (int o = 32; o > 0; o >>= 1) q += __shfl_xor(q, o);
  const float var = q * (1.0f / D_);
  const float mb = rbf(mean);
  const float sd = rbf(sqrtf(rbf(rbf(var) + 1e-5f)));
  unsigned* o = xn + (size_t)row * (D_ / 2);
#pragma unroll
  for (int i = 0; i < 5; ++i) {
    float l = rbf(a[2 * i] - mb) / sd;
    float h = rbf(a[2 * i + 1] - mb) / sd;
    o[lane + 64 * i] = pack2(l, h);
  }
  STAMP((char*)xn + (70u << 20), 1);
}

constexpr int N_P2 = 8 * 34 * 640;
__device__ __forceinline__ const char* p2_src(int i, const char* wo, const char* w2) {
  const int ct = i / (34 * 640), cc = (i / 640) % 34, j = i % 640;
  const int kk = j / 320, jj = j % 320;
  const int bi = jj >> 3, q = (jj >> 1) & 3, h = jj & 1;
  const int ghi = bi / 20, U = (bi % 20) >> 2, pos = bi & 3;
  const int k = 64 * cc + 32 * kk + 4 * (4 * ghi + 2 * (pos & 1) + (pos >> 1)) + q;
  return (k < D_ ? wo + (size_t)k * D_ * 2 : w2 + (size_t)(k - D_) * D_ * 2) + (80 * ct + 16 * U + 8 * h) * 2;
}
struct PackRegs { u32x4 v0, v1; };
__device__ __forceinline__ void pack_load(PackRegs& p, const char* wo, const char* w2) {
  const int i0 = blockIdx.x * 680 + threadIdx.x;
  p.v0 = *(const u32x4*)p2_src(i0, wo, w2);
  p.v1 = *(const u32x4*)p2_src(i0 + (threadIdx.x < 168 ? 512 : 0), wo, w2);
}
__device__ __forceinline__ void pack_store(const PackRegs& p, char* ws) {
  const unsigned i0 = blockIdx.x * 680 + threadIdx.x;
  *(u32x4*)(ws + WS_P2 + (size_t)i0 * 16) = p.v0;
  *(u32x4*)(ws + WS_P2 + (size_t)(i0 + (threadIdx.x < 168 ? 512 : 0)) * 16) = p.v1;
}

constexpr int G1_LDS = 147456;

struct GuCtx {
  const char* a_src;
  const char* b_src[3];
  int a_rd;
  int b_rd[6];
  int wid;
};

template <int T, int J>
__device__ __forceinline__ void gu_issue1(char* smem, const GuCtx& c) {
  constexpr int ST = (T + 4) / 2;
  constexpr int HF = (T + 4) & 1;
  constexpr int BT = T + 3;
  if constexpr (J == 0) {
    if constexpr (ST < 10)
      glds16(c.a_src + (size_t)HF * 64 * D_ * 2 + ST * 128, smem + (ST % 3) * 16384 + (HF * 64 + c.wid * 8) * 128);
  } else {
    if constexpr (BT >= 0 && BT < 20)
      glds16(c.b_src[J - 1] + (size_t)BT * 32 * F_ * 2, smem + 49152 + (BT & 3) * 24576 + (c.wid * 3 + J - 1) * 1024);
  }
}
template <int T>
__device__ __forceinline__ void gu_issue(char* smem, const GuCtx& c) {
  gu_issue1<T, 0>(smem, c); gu_issue1<T, 1>(smem, c); gu_issue1<T, 2>(smem, c); gu_issue1<T, 3>(smem, c);
}
template <int T> constexpr int gu_gsize() {
  return (T < -4) ? 0 : (((T + 4) / 2 < 10) ? 1 : 0) + ((T + 3 >= 0 && T + 3 < 20) ? 3 : 0);
}

struct GuFrag { bf16x8 a[4]; bf16x4 bl[6], bh[6]; };

template <int T, int I0>
__device__ __forceinline__ void gu_side(char* smem, unsigned ab, unsigned bb, const GuCtx& c, GuFrag& cur) {
  if constexpr (T < 20) {
    if constexpr (I0 % 6 == 3) gu_issue1<T, I0 / 6>(smem, c);
    else {
      constexpr int I = I0 - (I0 + 2) / 6;
      if constexpr (I < 4) rd128_issue<((T / 2) % 3) * 16384 + I * 2048>(cur.a[I], ab);
      else if constexpr (I < 16) {
        constexpr int n = (I - 4) >> 1;
        if constexpr (((I - 4) & 1) == 0) tr_issue<(T & 1) * 24576>(cur.bl[n], bb + c.b_rd[n]);
        else tr_issue<(T & 1) * 24576 + 3072>(cur.bh[n], bb + c.b_rd[n]);
      }
    }
  }
}
template <int T, int I>
__device__ __forceinline__ void gu_slots(char* smem, unsigned ab, unsigned bb, const GuCtx& c, f32x4 (&acc)[4][6],
                                         GuFrag& prev, GuFrag& cur) {
  if constexpr (T > 0) {
    constexpr int m = I / 6, n = I % 6;
    acc[m][n] = __builtin_amdgcn_mfma_f32_16x16x32_bf16(cat8(prev.bl[n], prev.bh[n]), prev.a[m], acc[m][n], 0, 0, 0);
  }
  gu_side<T, I>(smem, ab, bb, c, cur);
  __builtin_amdgcn_sched_barrier(0);
  if constexpr (I + 1 < 24) gu_slots<T, I + 1>(smem, ab, bb, c, acc, prev, cur);
}

template <int T>
__device__ __forceinline__ void gu_step(char* smem, unsigned lbase, const GuCtx& c, f32x4 (&acc)[4][6],
                                        GuFrag& prev, GuFrag& cur) {
  __builtin_amdgcn_sched_barrier(0);
  if constexpr (T > 0) { lgkm_pin(prev.bl, prev.bh); pin(prev.a); }
  if constexpr (T < 20) {
    wait_vm<gu_gsize<T - 2>() + gu_gsize<T - 1>()>();
    __builtin_amdgcn_s_barrier();
  }
  const unsigned bb = lbase + 49152 + ((T & 3) >> 1) * 49152;
  const unsigned ab = lbase + ((T & 1) ? (c.a_rd ^ 64) : c.a_rd);
  gu_slots<T, 0>(smem, ab, bb, c, acc, prev, cur);
  if constexpr (T < 20) gu_step<T + 1>(smem, lbase, c, acc, cur, prev);
}

__device__ __forceinline__ void gu_block(char* smem, int rt, int slice, const char* x, const char* w1,
                                         const char* w3, const char* wo, const char* w2, char* ws) {
  const int tid = threadIdx.x, lane = tid & 63;
  const int wid = __builtin_amdgcn_readfirstlane(tid >> 6);
  const int wm = wid >> 2, wn = wid & 3;
  const int row0 = rt * 128, f0 = slice * 192;
  char* oh = ws + WS_OH;
  PackRegs pk;
  pack_load(pk, wo, w2);
  GuCtx c;
  c.wid = wid;
  {
    const int r = lane >> 3, ch = (lane & 7) ^ r;
    c.a_src = x + ((size_t)(row0 + wid * 8 + r) * D_ + ch * 8) * 2;
  }
#pragma unroll
  for (int j = 0; j < 3; ++j) {
    const int off = (wid * 3 + j) * 1024 + lane * 16;
    const int k = off / 768, rem = off % 768;
    const int seg = rem >> 8, up = (rem >> 5) & 7, h = (rem >> 4) & 1;
    const int f = (k & 3) | (((k >> 3) & 1) << 2);
    const int col = (seg * 8 + (up ^ f)) * 16 + h * 8;
    const char* w = col < 192 ? w1 : w3;
    const int cc = col < 192 ? col : col - 192;
    c.b_src[j] = w + ((size_t)k * F_ + f0 + cc) * 2;
  }
  const int g = lane >> 4, q = (lane >> 2) & 3, p = lane & 3, fr = lane & 15;
  c.a_rd = (wm * 64 + fr) * 128 + ((g ^ (lane & 7)) << 4);
#pragma unroll
  for (int n = 0; n < 6; ++n) {
    const int U = (n < 3) ? 3 * wn + n : 12 + 3 * wn + (n - 3);
    const int f = q | ((g & 1) << 2);
    c.b_rd[n] = (8 * g + q) * 768 + (U >> 3) * 256 + (((U & 7) ^ f) << 5) + p * 8;
  }
  f32x4 acc[4][6];
#pragma unroll
  for (int m = 0; m < 4; ++m)
#pragma unroll
    for (int n = 0; n < 6; ++n) acc[m][n] = f32x4{0.f, 0.f, 0.f, 0.f};

  STAMP(oh + (64u << 20), 1);
  gu_issue<-4>(smem, c);
  gu_issue<-3>(smem, c);
  gu_issue<-2>(smem, c);
  gu_issue<-1>(smem, c);
  pack_store(pk, ws);
  {
    GuFrag f0_, f1_;
    gu_step<0>(smem, (unsigned)(size_t)smem, c, acc, f0_, f1_);
  }
  STAMP(oh + (64u << 20), 2);
  __builtin_amdgcn_s_barrier();
#pragma unroll
  for (int m = 0; m < 4; ++m) {
#pragma unroll
    for (int n = 0; n < 3; ++n) {
      u32x2 o;
#pragma unroll
      for (int r = 0; r < 2; ++r) {
        const f32x2 gt = rbf2(f32x2{acc[m][n][2 * r], acc[m][n][2 * r + 1]});
        const f32x2 up = rbf2(f32x2{acc[m][n + 3][2 * r], acc[m][n + 3][2 * r + 1]});
        const f32x2 t = gt * -1.4426950408889634f;
        f32x2 e = {__builtin_amdgcn_exp2f(t[0]), __builtin_amdgcn_exp2f(t[1])};
        e += 1.0f;
        const f32x2 rc = {__builtin_amdgcn_rcpf(e[0]), __builtin_amdgcn_rcpf(e[1])};
        const f32x2 hv = rbf2(gt * rc) * up;
        o[r] = pack2(hv[0], hv[1]);
      }
      *(u32x2*)(smem + (wm * 64 + m * 16 + fr) * 400 + (wn * 48 + n * 16 + g * 4) * 2) = o;
    }
  }
  __syncthreads();
  const rsrc_t ro = mk_rsrc(oh, S_ * KOH * 2);
#pragma unroll
  for (int i = 0; i < 6; ++i) {
    const int idx = tid + 512 * i;
    const int rr = idx / 24, ch = idx % 24;
    const u32x4 v = *(const u32x4*)(smem + rr * 400 + ch * 16);
    st16(ro, ((row0 + rr) * KOH + D_ + f0) * 2 + ch * 16, v);
  }
}

struct QkvCtx {
  const char* a_src;
  const char* ah_src;
  const char* ah_src1;
  const char* b_src[2];
  int a_rd;
  int b_rd[4];
  int wid;
};
constexpr int QA_STAGE = 144 * 128;
constexpr int QB_BASE = 3 * QA_STAGE;
constexpr int QB_SLOT = 32 * 512;

template <int T, int J>
__device__ __forceinline__ void qkv_issue1(char* smem, const QkvCtx& c) {
  constexpr int ST = (T + 4) / 2;
  constexpr int HF = (T + 4) & 1;
  constexpr int BT = T + 3;
  if constexpr (J == 0) {
    if constexpr (ST < 10)
      glds16(c.a_src + (size_t)HF * 64 * D_ * 2 + ST * 128,
             smem + (ST % 3) * QA_STAGE + (16 + HF * 64 + c.wid * 8) * 128);
  } else if constexpr (J == 1) {
    if constexpr (ST < 10)
      glds16_lo((HF ? c.ah_src1 : c.ah_src) + ST * 128,
                (unsigned)(size_t)smem + (ST % 3) * QA_STAGE + (HF * 8 + c.wid) * 128, 0xffull);
  } else {
    if constexpr (BT >= 0 && BT < 20)
      glds16(c.b_src[J - 2] + (size_t)BT * 32 * (3 * D_) * 2,
             smem + QB_BASE + (BT & 3) * QB_SLOT + (c.wid * 2 + J - 2) * 1024);
  }
}
template <int T>
__device__ __forceinline__ void qkv_issue(char* smem, const QkvCtx& c) {
  qkv_issue1<T, 0>(smem, c); qkv_issue1<T, 1>(smem, c); qkv_issue1<T, 2>(smem, c); qkv_issue1<T, 3>(smem, c);
}
template <int T> constexpr int qkv_gsize() {
  return (T < -4) ? 0 : (((T + 4) / 2 < 10) ? 2 : 0) + ((T + 3 >= 0 && T + 3 < 20) ? 2 : 0);
}

template <int MT> struct QkvFrag { bf16x8 a[MT]; bf16x4 bl[4], bh[4]; };

template <int T, int MT, int I0>
__device__ __forceinline__ void qkv_side(char* smem, unsigned ab, unsigned bb, const QkvCtx& c, QkvFrag<MT>& cur) {
  if constexpr (T < 20) {
    if constexpr (I0 % MT == 2) qkv_issue1<T, I0 / MT>(smem, c);
    else {
      constexpr int I = I0 - (I0 + MT - 3) / MT;
      if constexpr (I < MT) rd128_issue<((T / 2) % 3) * QA_STAGE + I * 2048>(cur.a[I], ab);
      else if constexpr (I < MT + 8) {
        constexpr int n = (I - MT) >> 1;
        if constexpr (((I - MT) & 1) == 0) tr_issue<(T & 1) * QB_SLOT>(cur.bl[n], bb + c.b_rd[n]);
        else tr_issue<(T & 1) * QB_SLOT + 2048>(cur.bh[n], bb + c.b_rd[n]);
      }
    }
  }
}
template <int T, int MT, int I>
__device__ __forceinline__ void qkv_slots(char* smem, unsigned ab, unsigned bb, const QkvCtx& c, f32x4 (&acc)[MT][4],
                                          QkvFrag<MT>& prev, QkvFrag<MT>& cur) {
  if constexpr (T > 0) {
    constexpr int m = I / 4, n = I % 4;
    acc[m][n] = __builtin_amdgcn_mfma_f32_16x16x32_bf16(cat8(prev.bl[n], prev.bh[n]), prev.a[m], acc[m][n], 0, 0, 0);
  }
  qkv_side<T, MT, I>(smem, ab, bb, c, cur);
  __builtin_amdgcn_sched_barrier(0);
  if constexpr (I + 1 < MT * 4) qkv_slots<T, MT, I + 1>(smem, ab, bb, c, acc, prev, cur);
}

template <int T, int MT>
__device__ __forceinline__ void qkv_step(char* smem, unsigned lbase, const QkvCtx& c, f32x4 (&acc)[MT][4],
                                         QkvFrag<MT>& prev, QkvFrag<MT>& cur) {
  __builtin_amdgcn_sched_barrier(0);
  if constexpr (T > 0) { lgkm_pin(prev.bl, prev.bh); pin(prev.a); }
  if constexpr (T < 20) {
    wait_vm<qkv_gsize<T - 2>() + qkv_gsize<T - 1>()>();
    __builtin_amdgcn_s_barrier();
  }
  const unsigned ab = lbase + ((T & 1) ? (c.a_rd ^ 64) : c.a_rd);
  const unsigned bb = lbase + QB_BASE + ((T & 3) >> 1) * 2 * QB_SLOT;
  qkv_slots<T, MT, 0>(smem, ab, bb, c, acc, prev, cur);
  if constexpr (T < 20) qkv_step<T + 1, MT>(smem, lbase, c, acc, cur, prev);
}

constexpr int QE_STRIDE = 488;

template <int MT>
__device__ __forceinline__ void qkv_main(char* smem, const QkvCtx& c, int lane, int arow0, int wn) {
  f32x4 acc[MT][4];
#pragma unroll
  for (int m = 0; m < MT; ++m)
#pragma unroll
    for (int n = 0; n < 4; ++n) acc[m][n] = f32x4{0.f, 0.f, 0.f, 0.f};
  {
    QkvFrag<MT> f0_, f1_;
    qkv_step<0, MT>(smem, (unsigned)(size_t)smem, c, acc, f0_, f1_);
  }
  __builtin_amdgcn_s_barrier();
  const int g = lane >> 4, fr = lane & 15;
#pragma unroll
  for (int n = 0; n < 4; ++n) {
    const int U = wn * 4 + n;
    if (U < 15) {
#pragma unroll
      for (int m = 0; m < MT; ++m) {
        u32x2 o;
        o[0] = pack2(acc[m][n][0], acc[m][n][1]);
        o[1] = pack2(acc[m][n][2], acc[m][n][3]);
        *(u32x2*)(smem + (arow0 + m * 16 + fr) * QE_STRIDE + U * 32 + g * 8) = o;
      }
    }
  }
}

__device__ __forceinline__ void qkv_block(char* smem, int rt, int hg, const char* x, const char* wqkv,
                                          const char* wo, const char* w2, char* ws) {
  const int tid = threadIdx.x, lane = tid & 63;
  const int wid = __builtin_amdgcn_readfirstlane(tid >> 6);
  const int wm = wid >> 2, wn = wid & 3;
  const int s0 = rt * 128;
  char* oh = ws + WS_OH;
  PackRegs pk;
  pack_load(pk, wo, w2);
  QkvCtx c;
  c.wid = wid;
  {
    const int r = lane >> 3, ch = (lane & 7) ^ r;
    c.a_src = x + ((size_t)(s0 + wid * 8 + r) * D_ + ch * 8) * 2;
    const int chh = (lane & 7) ^ wid;
    int r0 = s0 - 16 + wid, r1 = s0 - 8 + wid;
    r0 = r0 < 0 ? 0 : r0;
    r1 = r1 < 0 ? 0 : r1;
    c.ah_src = x + ((size_t)r0 * D_ + chh * 8) * 2;
    c.ah_src1 = x + ((size_t)r1 * D_ + chh * 8) * 2;
  }
#pragma unroll
  for (int j = 0; j < 2; ++j) {
    const int off = (wid * 2 + j) * 1024 + lane * 16;
    const int k = off >> 9, rem = off & 511;
    const int seg = rem >> 8, up = (rem >> 5) & 7, h = (rem >> 4) & 1;
    const int f = (k & 3) | (((k >> 3) & 1) << 2);
    int U = seg * 8 + (up ^ f);
    U = U > 14 ? 14 : U;
    const int sec = U / 5, uu = U % 5;
    c.b_src[j] = wqkv + ((size_t)k * (3 * D_) + sec * D_ + hg * 80 + uu * 16 + h * 8) * 2;
  }
  const int g = lane >> 4, q = (lane >> 2) & 3, p = lane & 3, fr = lane & 15;
  const int arow0 = wm ? 80 : 0;
  c.a_rd = (arow0 + fr) * 128 + ((g ^ (lane & 7)) << 4);
#pragma unroll
  for (int n = 0; n < 4; ++n) {
    const int U = wn * 4 + n;
    const int f = q | ((g & 1) << 2);
    c.b_rd[n] = (8 * g + q) * 512 + (U >> 3) * 256 + (((U & 7) ^ f) << 5) + p * 8;
  }
  STAMP(oh + (64u << 20), 1);
  qkv_issue<-4>(smem, c);
  qkv_issue<-3>(smem, c);
  qkv_issue<-2>(smem, c);
  qkv_issue<-1>(smem, c);
  pack_store(pk, ws);
  if (wm == 0) qkv_main<5>(smem, c, lane, arow0, wn);
  else qkv_main<4>(smem, c, lane, arow0, wn);
  __syncthreads();
  STAMP(oh + (64u << 20), 2);

  const int hh = tid >> 7, r = tid & 127;
  const int s = s0 + r;
  const char* qp = smem + (16 + r) * QE_STRIDE + hh * 40;
  unsigned qv[10];
#pragma unroll
  for (int i = 0; i < 5; ++i) {
    u32x2 t = *(const u32x2*)(qp + i * 8);
    qv[2 * i] = t[0]; qv[2 * i + 1] = t[1];
  }
  float lg[8];
  float mx = -3.0e38f;
#pragma unroll
  for (int w = 0; w < 8; ++w) {
    const char* kp = qp + 160 - 2 * w * QE_STRIDE;
    float d = 0.f;
#pragma unroll
    for (int i = 0; i < 5; ++i) {
      u32x2 t = *(const u32x2*)(kp + i * 8);
      d = dot2(qv[2 * i], t[0], d);
      d = dot2(qv[2 * i + 1], t[1], d);
    }
    float l = rbf(rbf(d) * 0.22360679774997896f);
    l = (s - 2 * w >= 0) ? l : rbf(-1e9f);
    lg[w] = l;
    mx = fmaxf(mx, l);
  }
  float sum = 0.f;
#pragma unroll
  for (int w = 0; w < 8; ++w) { lg[w] = __expf(lg[w] - mx); sum += lg[w]; }
  const float inv = 1.0f / sum;
  f32x2 o[10];
#pragma unroll
  for (int i = 0; i < 10; ++i) o[i] = f32x2{0.f, 0.f};
#pragma unroll
  for (int w = 0; w < 8; ++w) {
    const float pw = rbf(lg[w] * inv);
    const char* vp = qp + 320 - 2 * w * QE_STRIDE;
#pragma unroll
    for (int i = 0; i < 5; ++i) {
      u32x2 t = *(const u32x2*)(vp + i * 8);
      const unsigned t0 = t[0], t1 = t[1];
      o[2 * i] += pw * f32x2{bflo(t0), bfhi(t0)};
      o[2 * i + 1] += pw * f32x2{bflo(t1), bfhi(t1)};
    }
  }
#pragma unroll
  for (int i = 0; i < 5; ++i) {
    u32x2 t;
    t[0] = pack2(o[2 * i][0], o[2 * i][1]);
    t[1] = pack2(o[2 * i + 1][0], o[2 * i + 1][1]);
    *(u32x2*)(smem + (16 + r) * QE_STRIDE + hh * 40 + i * 8) = t;
  }
  __syncthreads();
  const rsrc_t ro = mk_rsrc(oh, S_ * KOH * 2);
#pragma unroll
  for (int i = 0; i < 5; ++i) {
    const int idx = tid + 512 * i;
    const int rr = idx / 20, part = idx % 20;
    u32x2 t = *(const u32x2*)(smem + (16 + rr) * QE_STRIDE + part * 8);
    st8(ro, ((s0 + rr) * KOH + hg * 80) * 2 + part * 8, t);
  }
}

__global__ __launch_bounds__(512, 2) void k_gemm1(const char* x, const char* __restrict__ wqkv,
                                                  const char* __restrict__ wo, const char* __restrict__ w1,
                                                  const char* __restrict__ w3, const char* __restrict__ w2,
                                                  char* ws) {
  __shared__ __attribute__((aligned(16))) char smem[G1_LDS];
  STAMP(ws + WS_OH + (64u << 20), 0);
  const int b = blockIdx.x;
  const int xc = b & 7, idx = b >> 3;
  const int rh = xc & 1, j = xc >> 1;
  const int i2 = idx & 15;
  const int rt = rh * 8 + (i2 >> 1), cs = 2 * j + (i2 & 1);
  if (idx < 16) gu_block(smem, rt, cs, x, w1, w3, wo, w2, ws);
  else qkv_block(smem, rt, cs, x, wqkv, wo, w2, ws);
  STAMP(ws + WS_OH + (64u << 20), 3);
}

constexpr int G2_STAGE = 8192 + 10240;
constexpr int G2_LDS = 8 * G2_STAGE;
constexpr int G2_NC = KOH / 64;

struct G2Ctx {
  const char* a_src;
  const char* b_src;
  int a_rd, b_rd;
  int wid;
};
struct G2Frag { bf16x8 a; bf16x4 bl[5], bh[5]; };

template <int C>
__device__ __forceinline__ void g2_issue(char* smem, const G2Ctx& c) {
  if constexpr (C < G2_NC) {
    char* dst = smem + (C & 7) * G2_STAGE;
    glds16(c.a_src + C * 128, dst + c.wid * 1024);
    glds16(c.b_src + C * 10240, dst + 8192 + c.wid * 1280);
    glds16_lo(c.b_src + C * 10240 + 1024, (unsigned)(size_t)dst + 8192 + c.wid * 1280 + 1024, 0xffffull);
  }
}
template <int C>
__device__ __forceinline__ void g2_step(char* smem, unsigned lbase, const G2Ctx& c, f32x4 (&accA)[5],
                                        f32x4 (&accB)[5], G2Frag& prev, G2Frag& cur) {
  __builtin_amdgcn_sched_barrier(0);
  if constexpr (C > 0) {
    lgkm_pin(prev.bl, prev.bh);
    asm volatile("" : "+v"(prev.a));
  }
  if constexpr (C < G2_NC) {
    constexpr int ahead = (G2_NC - 1 - C) < 6 ? (G2_NC - 1 - C) : 6;
    wait_vm<3 * ahead>();
    __builtin_amdgcn_s_barrier();
    const unsigned sb = lbase + (C & 7) * G2_STAGE;
    rd128_issue<0>(cur.a, sb + c.a_rd);
    const unsigned ba = sb + c.b_rd;
    tr_issue<0>(cur.bl[0], ba);    tr_issue<256>(cur.bh[0], ba);
    tr_issue<512>(cur.bl[1], ba);  tr_issue<512 + 256>(cur.bh[1], ba);
    tr_issue<1024>(cur.bl[2], ba); tr_issue<1024 + 256>(cur.bh[2], ba);
    tr_issue<1536>(cur.bl[3], ba); tr_issue<1536 + 256>(cur.bh[3], ba);
    tr_issue<2048>(cur.bl[4], ba); tr_issue<2048 + 256>(cur.bh[4], ba);
    __builtin_amdgcn_sched_barrier(0);
  }
  if constexpr (C > 0) {
#pragma unroll
    for (int n = 0; n < 5; ++n) {
      if constexpr (C - 1 < 10)
        accA[n] = __builtin_amdgcn_mfma_f32_16x16x32_bf16(cat8(prev.bl[n], prev.bh[n]), prev.a, accA[n], 0, 0, 0);
      else
        accB[n] = __builtin_amdgcn_mfma_f32_16x16x32_bf16(cat8(prev.bl[n], prev.bh[n]), prev.a, accB[n], 0, 0, 0);
    }
  }
  g2_issue<C + 7>(smem, c);
  if constexpr (C < G2_NC) g2_step<C + 1>(smem, lbase, c, accA, accB, cur, prev);
}

__global__ __launch_bounds__(512, 2) void k_gemm2(const char* __restrict__ ws, const char* __restrict__ xin,
                                                  char* __restrict__ out) {
  __shared__ __attribute__((aligned(16))) char smem[G2_LDS];
  const char* oh = ws + WS_OH;
  const int tid = threadIdx.x, lane = tid & 63;
  const int wid = __builtin_amdgcn_readfirstlane(tid >> 6);
  const int b = blockIdx.x;
  const int x = b & 7, idx = b >> 3;
  const int rt = (x >> 1) * 8 + (idx >> 2), ct = (x & 1) * 4 + (idx & 3);
  const int row0 = rt * 64, col0 = ct * 80;
  const int kk = wid >> 2, mq = wid & 3;
  STAMP((char*)oh + (65u << 20), 0);
  G2Ctx c;
  c.wid = wid;
  {
    const int r = lane >> 3, ch = (lane & 7) ^ r;
    c.a_src = oh + ((size_t)(row0 + wid * 8 + r) * KOH + ch * 8) * 2;
  }
  c.b_src = ws + WS_P2 + (size_t)ct * 34 * 10240 + wid * 1280 + lane * 16;
  const int g = lane >> 4, q = (lane >> 2) & 3, p = lane & 3, fr = lane & 15;
  c.a_rd = (mq * 16 + fr) * 128 + (((kk * 4 + g) ^ (lane & 7)) << 4);
  c.b_rd = 8192 + kk * 5120 + ((g >> 1) * 20 + (g & 1)) * 128 + q * 32 + p * 8;

  f32x4 accA[5], accB[5];
#pragma unroll
  for (int n = 0; n < 5; ++n) { accA[n] = f32x4{0.f, 0.f, 0.f, 0.f}; accB[n] = accA[n]; }
  STAMP((char*)oh + (65u << 20), 1);
  g2_issue<0>(smem, c); g2_issue<1>(smem, c); g2_issue<2>(smem, c); g2_issue<3>(smem, c);
  g2_issue<4>(smem, c); g2_issue<5>(smem, c); g2_issue<6>(smem, c);
  {
    G2Frag f0_, f1_;
    g2_step<0>(smem, (unsigned)(size_t)smem, c, accA, accB, f0_, f1_);
  }
  STAMP((char*)oh + (65u << 20), 2);
  const size_t go = ((size_t)(row0 + mq * 16 + fr) * D_ + col0 + g * 4) * 2;
  u32x2 xv[3];
  if (kk == 0) {
#pragma unroll
    for (int n = 0; n < 3; ++n) xv[n] = *(const u32x2*)(xin + go + n * 32);
  } else {
#pragma unroll
    for (int n = 0; n < 2; ++n) xv[n] = *(const u32x2*)(xin + go + (n + 3) * 32);
    xv[2] = xv[1];
  }
  __builtin_amdgcn_s_barrier();
  f32x4* ex = (f32x4*)smem + wid * 6 * 64 + lane;
  if (kk == 0) {
#pragma unroll
    for (int n = 0; n < 2; ++n) { ex[n * 64] = accA[n + 3]; ex[(3 + n) * 64] = accB[n + 3]; }
  } else {
#pragma unroll
    for (int n = 0; n < 3; ++n) { ex[n * 64] = accA[n]; ex[(3 + n) * 64] = accB[n]; }
  }
  __syncthreads();
  const f32x4* pe = (const f32x4*)smem + (wid ^ 4) * 6 * 64 + lane;
  auto fin = [&](f32x4 a, f32x4 bq, u32x2 xx, int n) {
    const unsigned x0 = xx[0], x1 = xx[1];
    const float xs[4] = {bflo(x0), bfhi(x0), bflo(x1), bfhi(x1)};
    float r[4];
#pragma unroll
    for (int e = 0; e < 4; ++e) r[e] = rbf(xs[e] + rbf(a[e])) + rbf(bq[e]);
    u32x2 o;
    o[0] = pack2(r[0], r[1]);
    o[1] = pack2(r[2], r[3]);
    *(u32x2*)(out + go + n * 32) = o;
  };
  if (kk == 0) {
#pragma unroll
    for (int n = 0; n < 3; ++n) fin(accA[n] + pe[n * 64], accB[n] + pe[(3 + n) * 64], xv[n], n);
  } else {
#pragma unroll
    for (int n = 0; n < 2; ++n) fin(pe[n * 64] + accA[n + 3], pe[(3 + n) * 64] + accB[n + 3], xv[n], n + 3);
  }
  STAMP((char*)oh + (65u << 20), 3);
}

extern "C" void kernel_launch(void* const* d_in, const int* in_sizes, int n_in, void* d_out, int out_size,
                              void* d_ws, size_t ws_size, hipStream_t stream) {
  const char* x = (const char*)d_in[0];
  const char* wqkv = (const char*)d_in[1];
  const char* wo = (const char*)d_in[2];
  const char* w1 = (const char*)d_in[4];
  const char* w3 = (const char*)d_in[5];
  const char* w2 = (const char*)d_in[6];
  char* ws = (char*)d_ws;
  k_ln<<<S_ / 4, 256, 0, stream>>>((const unsigned*)x, (unsigned*)ws);
  k_gemm1<<<256, 512, 0, stream>>>(ws, wqkv, wo, w1, w3, w2, ws);
  k_gemm2<<<256, 512, 0, stream>>>(ws, x, (char*)d_out);
}
```

```cpp
#include <hip/hip_runtime.h>
#include <hip/hip_bf16.h>
#include <stdint.h>
#include <type_traits>

typedef __attribute__((ext_vector_type(8))) short bf16x8;
typedef __attribute__((ext_vector_type(4))) short bf16x4;
typedef __attribute__((ext_vector_type(4))) float f32x4;
typedef __attribute__((ext_vector_type(2))) float f32x2;
typedef __attribute__((ext_vector_type(2))) __bf16 bf2;
typedef __attribute__((ext_vector_type(2))) unsigned u32x2;
typedef __attribute__((ext_vector_type(4))) unsigned u32x4;

#define STAMP(base, i)
constexpr int S_ = 2048, D_ = 640, F_ = 1536;
constexpr int KOH = D_ + F_;
constexpr unsigned WS_OH = 4u << 20, WS_P2 = 24u << 20;

__device__ __forceinline__ float rbf(float f) { return (float)(__bf16)f; }
__device__ __forceinline__ unsigned pack2(float lo, float hi) {
  bf2 v; v[0] = (__bf16)lo; v[1] = (__bf16)hi;
  return __builtin_bit_cast(unsigned, v);
}
__device__ __forceinline__ float bflo(unsigned u) { return __builtin_bit_cast(float, u << 16); }
__device__ __forceinline__ float bfhi(unsigned u) { return __builtin_bit_cast(float, u & 0xffff0000u); }
__device__ __forceinline__ f32x2 rbf2(f32x2 v) {
  const unsigned u = pack2(v[0], v[1]);
  return f32x2{bflo(u), bfhi(u)};
}
__device__ __forceinline__ float dot2(unsigned a, unsigned b, float c) {
  return __builtin_amdgcn_fdot2_f32_bf16(__builtin_bit_cast(bf2, a), __builtin_bit_cast(bf2, b), c, false);
}
#define ST_AUX 16
typedef __amdgpu_buffer_rsrc_t rsrc_t;
__device__ __forceinline__ rsrc_t mk_rsrc(void* p, unsigned bytes) {
  return __builtin_amdgcn_make_buffer_rsrc(p, 0, bytes, 0x00020000);
}
__device__ __forceinline__ void st16(rsrc_t r, unsigned off, u32x4 v) {
  __builtin_amdgcn_raw_buffer_store_b128(v, r, off, 0, ST_AUX);
}
__device__ __forceinline__ void st8(rsrc_t r, unsigned off, u32x2 v) {
  __builtin_amdgcn_raw_buffer_store_b64(v, r, off, 0, ST_AUX);
}
template <int N> __device__ __forceinline__ void wait_vm() {
  asm volatile("s_waitcnt vmcnt(%0)" ::"i"(N) : "memory");
}
__device__ __forceinline__ void glds16(const void* g, char* l) {
  __builtin_amdgcn_global_load_lds((const unsigned*)g, (unsigned*)l, 16, 0, 0);
}
__device__ __forceinline__ void glds16_lo(const void* g, unsigned lds_addr, unsigned long long mask) {
  unsigned keep;
  asm volatile(
      "s_mov_b32 %0, m0\n\ts_mov_b32 m0, %2\n\ts_mov_b64 exec, %3\n\tglobal_load_lds_dwordx4 %1, off\n\t"
      "s_mov_b64 exec, -1\n\ts_mov_b32 m0, %0"
      : "=&s"(keep) : "v"(g), "s"(lds_addr), "s"(mask) : "memory");
}
template <int IMM>
__device__ __forceinline__ void tr_issue(bf16x4& d, unsigned addr) {
  asm volatile("ds_read_b64_tr_b16 %0, %1 offset:%2" : "=v"(d) : "v"(addr), "i"(IMM) : "memory");
}
template <int IMM>
__device__ __forceinline__ void rd128_issue(bf16x8& d, unsigned addr) {
  asm volatile("ds_read_b128 %0, %1 offset:%2" : "=v"(d) : "v"(addr), "i"(IMM) : "memory");
}
__device__ __forceinline__ void pin(bf16x8 (&a)[4]) {
  asm volatile("" : "+v"(a[0]), "+v"(a[1]), "+v"(a[2]), "+v"(a[3]));
}
__device__ __forceinline__ void pin(bf16x8 (&a)[5]) {
  asm volatile("" : "+v"(a[0]), "+v"(a[1]), "+v"(a[2]), "+v"(a[3]), "+v"(a[4]));
}
__device__ __forceinline__ void lgkm_pin(bf16x4 (&l)[4], bf16x4 (&h)[4]) {
  asm volatile("s_waitcnt lgkmcnt(0)"
               : "+v"(l[0]), "+v"(l[1]), "+v"(l[2]), "+v"(l[3]), "+v"(h[0]), "+v"(h[1]), "+v"(h[2]), "+v"(h[3])
               :: "memory");
}
__device__ __forceinline__ void lgkm_pin(bf16x4 (&l)[5], bf16x4 (&h)[5]) {
  asm volatile("s_waitcnt lgkmcnt(0)"
               : "+v"(l[0]), "+v"(l[1]), "+v"(l[2]), "+v"(l[3]), "+v"(l[4]), "+v"(h[0]), "+v"(h[1]), "+v"(h[2]),
                 "+v"(h[3]), "+v"(h[4])
               :: "memory");
}
__device__ __forceinline__ void lgkm_pin(bf16x4 (&l)[6], bf16x4 (&h)[6]) {
  asm volatile("s_waitcnt lgkmcnt(0)"
               : "+v"(l[0]), "+v"(l[1]), "+v"(l[2]), "+v"(l[3]), "+v"(l[4]), "+v"(l[5]), "+v"(h[0]), "+v"(h[1]),
                 "+v"(h[2]), "+v"(h[3]), "+v"(h[4]), "+v"(h[5])
               :: "memory");
}
__device__ __forceinline__ bf16x8 cat8(bf16x4 a, bf16x4 b) {
  return __builtin_shufflevector(a, b, 0, 1, 2, 3, 4, 5, 6, 7);
}

#define LN_W 4
__global__ __launch_bounds__(64 * LN_W) void k_ln(const unsigned* __restrict__ x, unsigned* __restrict__ xn) {
  STAMP((char*)xn + (70u << 20), 0);
  const int lane = threadIdx.x & 63;
  const int row = blockIdx.x * LN_W + (threadIdx.x >> 6);
  const unsigned* xr = x + (size_t)row * (D_ / 2);
  unsigned v[5];
#pragma unroll
  for (int i = 0; i < 5; ++i) v[i] = xr[lane + 64 * i];
  float a[10];
  float s = 0.f;
#pragma unroll
  for (int i = 0; i < 5; ++i) {
    a[2 * i] = bflo(v[i]); a[2 * i + 1] = bfhi(v[i]);
    s += a[2 * i] + a[2 * i + 1];
  }
#pragma unroll
  for (int o = 32; o > 0; o >>= 1) s += __shfl_xor(s, o);
  const float mean = s * (1.0f / D_);
  float q = 0.f;
#pragma unroll
  for (int i = 0; i < 10; ++i) { float d = a[i] - mean; q += d * d; }
#pragma unroll
  for (int o = 32; o > 0; o >>= 1) q += __shfl_xor(q, o);
  const float var = q * (1.0f / D_);
  const float mb = rbf(mean);
  const float sd = rbf(sqrtf(rbf(rbf(var) + 1e-5f)));
  unsigned* o = xn + (size_t)row * (D_ / 2);
#pragma unroll
  for (int i = 0; i < 5; ++i) {
    float l = rbf(a[2 * i] - mb) / sd;
    float h = rbf(a[2 * i + 1] - mb) / sd;
    o[lane + 64 * i] = pack2(l, h);
  }
  STAMP((char*)xn + (70u << 20), 1);
}

constexpr int N_P2 = 8 * 34 * 640;
__device__ __forceinline__ const char* p2_src(int i, const char* wo, const char* w2) {
  const int ct = i / (34 * 640), cc = (i / 640) % 34, j = i % 640;
  const int kk = j / 320, jj = j % 320;
  const int bi = jj >> 3, q = (jj >> 1) & 3, h = jj & 1;
  const int ghi = bi / 20, U = (bi % 20) >> 2, pos = bi & 3;
  const int k = 64 * cc + 32 * kk + 4 * (4 * ghi + 2 * (pos & 1) + (pos >> 1)) + q;
  return (k < D_ ? wo + (size_t)k * D_ * 2 : w2 + (size_t)(k - D_) * D_ * 2) + (80 * ct + 16 * U + 8 * h) * 2;
}
struct PackRegs { u32x4 v0, v1; };
__device__ __forceinline__ void pack_load(PackRegs& p, const char* wo, const char* w2) {
  const int i0 = blockIdx.x * 680 + threadIdx.x;
  p.v0 = *(const u32x4*)p2_src(i0, wo, w2);
  p.v1 = *(const u32x4*)p2_src(i0 + (threadIdx.x < 168 ? 512 : 0), wo, w2);
}
__device__ __forceinline__ void pack_store(const PackRegs& p, char* ws) {
  const unsigned i0 = blockIdx.x * 680 + threadIdx.x;
  *(u32x4*)(ws + WS_P2 + (size_t)i0 * 16) = p.v0;
  *(u32x4*)(ws + WS_P2 + (size_t)(i0 + (threadIdx.x < 168 ? 512 : 0)) * 16) = p.v1;
}

constexpr int G1_LDS = 147456;

struct GuCtx {
  const char* a_src;
  const char* b_src[3];
  int a_rd;
  int b_rd[6];
  int wid;
};

template <int T, int J>
__device__ __forceinline__ void gu_issue1(char* smem, const GuCtx& c) {
  constexpr int ST = (T + 4) / 2;
  constexpr int HF = (T + 4) & 1;
  constexpr int BT = T + 3;
  if constexpr (J == 0) {
    if constexpr (ST < 10)
      glds16(c.a_src + (size_t)HF * 64 * D_ * 2 + ST * 128, smem + (ST % 3) * 16384 + (HF * 64 + c.wid * 8) * 128);
  } else {
    if constexpr (BT >= 0 && BT < 20)
      glds16(c.b_src[J - 1] + (size_t)BT * 32 * F_ * 2, smem + 49152 + (BT & 3) * 24576 + (c.wid * 3 + J - 1) * 1024);
  }
}
template <int T>
__device__ __forceinline__ void gu_issue(char* smem, const GuCtx& c) {
  gu_issue1<T, 0>(smem, c); gu_issue1<T, 1>(smem, c); gu_issue1<T, 2>(smem, c); gu_issue1<T, 3>(smem, c);
}
template <int T> constexpr int gu_gsize() {
  return (T < -4) ? 0 : (((T + 4) / 2 < 10) ? 1 : 0) + ((T + 3 >= 0 && T + 3 < 20) ? 3 : 0);
}

struct GuFrag { bf16x8 a[4]; bf16x4 bl[6], bh[6]; };

template <int T, int I0>
__device__ __forceinline__ void gu_side(char* smem, unsigned ab, unsigned bb, const GuCtx& c, GuFrag& cur) {
  if constexpr (T < 20) {
    if constexpr (I0 % 6 == 3) gu_issue1<T, I0 / 6>(smem, c);
    else {
      constexpr int I = I0 - (I0 + 2) / 6;
      if constexpr (I < 4) rd128_issue<((T / 2) % 3) * 16384 + I * 2048>(cur.a[I], ab);
      else if constexpr (I < 16) {
        constexpr int n = (I - 4) >> 1;
        if constexpr (((I - 4) & 1) == 0) tr_issue<(T & 1) * 24576>(cur.bl[n], bb + c.b_rd[n]);
        else tr_issue<(T & 1) * 24576 + 3072>(cur.bh[n], bb + c.b_rd[n]);
      }
    }
  }
}
template <int T, int I>
__device__ __forceinline__ void gu_slots(char* smem, unsigned ab, unsigned bb, const GuCtx& c, f32x4 (&acc)[4][6],
                                         GuFrag& prev, GuFrag& cur) {
  if constexpr (T > 0) {
    constexpr int m = I / 6, n = I % 6;
    acc[m][n] = __builtin_amdgcn_mfma_f32_16x16x32_bf16(cat8(prev.bl[n], prev.bh[n]), prev.a[m], acc[m][n], 0, 0, 0);
  }
  gu_side<T, I>(smem, ab, bb, c, cur);
  __builtin_amdgcn_sched_barrier(0);
  if constexpr (I + 1 < 24) gu_slots<T, I + 1>(smem, ab, bb, c, acc, prev, cur);
}

template <int T>
__device__ __forceinline__ void gu_step(char* smem, unsigned lbase, const GuCtx& c, f32x4 (&acc)[4][6],
                                        GuFrag& prev, GuFrag& cur) {
  __builtin_amdgcn_sched_barrier(0);
  if constexpr (T > 0) { lgkm_pin(prev.bl, prev.bh); pin(prev.a); }
  if constexpr (T < 20) {
    wait_vm<gu_gsize<T - 2>() + gu_gsize<T - 1>()>();
    __builtin_amdgcn_s_barrier();
  }
  const unsigned bb = lbase + 49152 + ((T & 3) >> 1) * 49152;
  const unsigned ab = lbase + ((T & 1) ? (c.a_rd ^ 64) : c.a_rd);
  gu_slots<T, 0>(smem, ab, bb, c, acc, prev, cur);
  if constexpr (T < 20) gu_step<T + 1>(smem, lbase, c, acc, cur, prev);
}

__device__ __forceinline__ void gu_block(char* smem, int rt, int slice, const char* x, const char* w1,
                                         const char* w3, const char* wo, const char* w2, char* ws) {
  const int tid = threadIdx.x, lane = tid & 63;
  const int wid = __builtin_amdgcn_readfirstlane(tid >> 6);
  const int wm = wid >> 2, wn = wid & 3;
  const int row0 = rt * 128, f0 = slice * 192;
  char* oh = ws + WS_OH;
  PackRegs pk;
  pack_load(pk, wo, w2);
  GuCtx c;
  c.wid = wid;
  {
    const int r = lane >> 3, ch = (lane & 7) ^ r;
    c.a_src = x + ((size_t)(row0 + wid * 8 + r) * D_ + ch * 8) * 2;
  }
#pragma unroll
  for (int j = 0; j < 3; ++j) {
    const int off = (wid * 3 + j) * 1024 + lane * 16;
    const int k = off / 768, rem = off % 768;
    const int seg = rem >> 8, up = (rem >> 5) & 7, h = (rem >> 4) & 1;
    const int f = (k & 3) | (((k >> 3) & 1) << 2);
    const int col = (seg * 8 + (up ^ f)) * 16 + h * 8;
    const char* w = col < 192 ? w1 : w3;
    const int cc = col < 192 ? col : col - 192;
    c.b_src[j] = w + ((size_t)k * F_ + f0 + cc) * 2;
  }
  const int g = lane >> 4, q = (lane >> 2) & 3, p = lane & 3, fr = lane & 15;
  c.a_rd = (wm * 64 + fr) * 128 + ((g ^ (lane & 7)) << 4);
#pragma unroll
  for (int n = 0; n < 6; ++n) {
    const int U = (n < 3) ? 3 * wn + n : 12 + 3 * wn + (n - 3);
    const int f = q | ((g & 1) << 2);
    c.b_rd[n] = (8 * g + q) * 768 + (U >> 3) * 256 + (((U & 7) ^ f) << 5) + p * 8;
  }
  f32x4 acc[4][6];
#pragma unroll
  for (int m = 0; m < 4; ++m)
#pragma unroll
    for (int n = 0; n < 6; ++n) acc[m][n] = f32x4{0.f, 0.f, 0.f, 0.f};

  STAMP(oh + (64u << 20), 1);
  gu_issue<-4>(smem, c);
  gu_issue<-3>(smem, c);
  gu_issue<-2>(smem, c);
  gu_issue<-1>(smem, c);
  pack_store(pk, ws);
  {
    GuFrag f0_, f1_;
    gu_step<0>(smem, (unsigned)(size_t)smem, c, acc, f0_, f1_);
  }
  STAMP(oh + (64u << 20), 2);
  __builtin_amdgcn_s_barrier();
  const rsrc_t ro = mk_rsrc(oh, S_ * KOH * 2);
#pragma unroll
  for (int m = 0; m < 4; ++m) {
#pragma unroll
    for (int n = 0; n < 3; ++n) {
      u32x2 o;
#pragma unroll
      for (int r = 0; r < 2; ++r) {
        const f32x2 gt = rbf2(f32x2{acc[m][n][2 * r], acc[m][n][2 * r + 1]});
        const f32x2 up = rbf2(f32x2{acc[m][n + 3][2 * r], acc[m][n + 3][2 * r + 1]});
        const f32x2 t = gt * -1.4426950408889634f;
        f32x2 e = {__builtin_amdgcn_exp2f(t[0]), __builtin_amdgcn_exp2f(t[1])};
        e += 1.0f;
        const f32x2 rc = {__builtin_amdgcn_rcpf(e[0]), __builtin_amdgcn_rcpf(e[1])};
        const f32x2 hv = rbf2(gt * rc) * up;
        o[r] = pack2(hv[0], hv[1]);
      }
      *(u32x2*)(smem + (wm * 64 + m * 16 + fr) * 400 + (wn * 48 + n * 16 + g * 4) * 2) = o;
    }
  }
  __syncthreads();
#pragma unroll
  for (int i = 0; i < 6; ++i) {
    const int idx = tid + 512 * i;
    const int rr = idx / 24, ch = idx % 24;
    const u32x4 v = *(const u32x4*)(smem + rr * 400 + ch * 16);
    st16(ro, ((row0 + rr) * KOH + D_ + f0) * 2 + ch * 16, v);
  }
}

struct QkvCtx {
  const char* a_src;
  const char* ah_src;
  const char* ah_src1;
  const char* b_src[2];
  int a_rd;
  int b_rd[4];
  int wid;
};
constexpr int QA_STAGE = 144 * 128;
constexpr int QB_BASE = 3 * QA_STAGE;
constexpr int QB_SLOT = 32 * 512;

template <int T, int J>
__device__ __forceinline__ void qkv_issue1(char* smem, const QkvCtx& c) {
  constexpr int ST = (T + 4) / 2;
  constexpr int HF = (T + 4) & 1;
  constexpr int BT = T + 3;
  if constexpr (J == 0) {
    if constexpr (ST < 10)
      glds16(c.a_src + (size_t)HF * 64 * D_ * 2 + ST * 128,
             smem + (ST % 3) * QA_STAGE + (16 + HF * 64 + c.wid * 8) * 128);
  } else if constexpr (J == 1) {
    if constexpr (ST < 10)
      glds16_lo((HF ? c.ah_src1 : c.ah_src) + ST * 128,
                (unsigned)(size_t)smem + (ST % 3) * QA_STAGE + (HF * 8 + c.wid) * 128, 0xffull);
  } else {
    if constexpr (BT >= 0 && BT < 20)
      glds16(c.b_src[J - 2] + (size_t)BT * 32 * (3 * D_) * 2,
             smem + QB_BASE + (BT & 3) * QB_SLOT + (c.wid * 2 + J - 2) * 1024);
  }
}
template <int T>
__device__ __forceinline__ void qkv_issue(char* smem, const QkvCtx& c) {
  qkv_issue1<T, 0>(smem, c); qkv_issue1<T, 1>(smem, c); qkv_issue1<T, 2>(smem, c); qkv_issue1<T, 3>(smem, c);
}
template <int T> constexpr int qkv_gsize() {
  return (T < -4) ? 0 : (((T + 4) / 2 < 10) ? 2 : 0) + ((T + 3 >= 0 && T + 3 < 20) ? 2 : 0);
}

template <int MT> struct QkvFrag { bf16x8 a[MT]; bf16x4 bl[4], bh[4]; };

template <int T, int MT, int I0>
__device__ __forceinline__ void qkv_side(char* smem, unsigned ab, unsigned bb, const QkvCtx& c, QkvFrag<MT>& cur) {
  if constexpr (T < 20) {
    if constexpr (I0 % MT == 2) qkv_issue1<T, I0 / MT>(smem, c);
    else {
      constexpr int I = I0 - (I0 + MT - 3) / MT;
      if constexpr (I < MT) rd128_issue<((T / 2) % 3) * QA_STAGE + I * 2048>(cur.a[I], ab);
      else if constexpr (I < MT + 8) {
        constexpr int n = (I - MT) >> 1;
        if constexpr (((I - MT) & 1) == 0) tr_issue<(T & 1) * QB_SLOT>(cur.bl[n], bb + c.b_rd[n]);
        else tr_issue<(T & 1) * QB_SLOT + 2048>(cur.bh[n], bb + c.b_rd[n]);
      }
    }
  }
}
template <int T, int MT, int I>
__device__ __forceinline__ void qkv_slots(char* smem, unsigned ab, unsigned bb, const QkvCtx& c, f32x4 (&acc)[MT][4],
                                          QkvFrag<MT>& prev, QkvFrag<MT>& cur) {
  if constexpr (T > 0) {
    constexpr int m = I / 4, n = I % 4;
    acc[m][n] = __builtin_amdgcn_mfma_f32_16x16x32_bf16(cat8(prev.bl[n], prev.bh[n]), prev.a[m], acc[m][n], 0, 0, 0);
  }
  qkv_side<T, MT, I>(smem, ab, bb, c, cur);
  __builtin_amdgcn_sched_barrier(0);
  if constexpr (I + 1 < MT * 4) qkv_slots<T, MT, I + 1>(smem, ab, bb, c, acc, prev, cur);
}

template <int T, int MT>
__device__ __forceinline__ void qkv_step(char* smem, unsigned lbase, const QkvCtx& c, f32x4 (&acc)[MT][4],
                                         QkvFrag<MT>& prev, QkvFrag<MT>& cur) {
  __builtin_amdgcn_sched_barrier(0);
  if constexpr (T > 0) { lgkm_pin(prev.bl, prev.bh); pin(prev.a); }
  if constexpr (T < 20) {
    wait_vm<qkv_gsize<T - 2>() + qkv_gsize<T - 1>()>();
    __builtin_amdgcn_s_barrier();
  }
  const unsigned ab = lbase + ((T & 1) ? (c.a_rd ^ 64) : c.a_rd);
  const unsigned bb = lbase + QB_BASE + ((T & 3) >> 1) * 2 * QB_SLOT;
  qkv_slots<T, MT, 0>(smem, ab, bb, c, acc, prev, cur);
  if constexpr (T < 20) qkv_step<T + 1, MT>(smem, lbase, c, acc, cur, prev);
}

constexpr int QE_STRIDE = 488;

template <int MT>
__device__ __forceinline__ void qkv_main(char* smem, const QkvCtx& c, int lane, int arow0, int wn) {
  f32x4 acc[MT][4];
#pragma unroll
  for (int m = 0; m < MT; ++m)
#pragma unroll
    for (int n = 0; n < 4; ++n) acc[m][n] = f32x4{0.f, 0.f, 0.f, 0.f};
  {
    QkvFrag<MT> f0_, f1_;
    qkv_step<0, MT>(smem, (unsigned)(size_t)smem, c, acc, f0_, f1_);
  }
  __builtin_amdgcn_s_barrier();
  const int g = lane >> 4, fr = lane & 15;
#pragma unroll
  for (int n = 0; n < 4; ++n) {
    const int U = wn * 4 + n;
    if (U < 15) {
#pragma unroll
      for (int m = 0; m < MT; ++m) {
        u32x2 o;
        o[0] = pack2(acc[m][n][0], acc[m][n][1]);
        o[1] = pack2(acc[m][n][2], acc[m][n][3]);
        *(u32x2*)(smem + (arow0 + m * 16 + fr) * QE_STRIDE + U * 32 + g * 8) = o;
      }
    }
  }
}

__device__ __forceinline__ void qkv_block(char* smem, int rt, int hg, const char* x, const char* wqkv,
                                          const char* wo, const char* w2, char* ws) {
  const int tid = threadIdx.x, lane = tid & 63;
  const int wid = __builtin_amdgcn_readfirstlane(tid >> 6);
  const int wm = wid >> 2, wn = wid & 3;
  const int s0 = rt * 128;
  char* oh = ws + WS_OH;
  PackRegs pk;
  pack_load(pk, wo, w2);
  QkvCtx c;
  c.wid = wid;
  {
    const int r = lane >> 3, ch = (lane & 7) ^ r;
    c.a_src = x + ((size_t)(s0 + wid * 8 + r) * D_ + ch * 8) * 2;
    const int chh = (lane & 7) ^ wid;
    int r0 = s0 - 16 + wid, r1 = s0 - 8 + wid;
    r0 = r0 < 0 ? 0 : r0;
    r1 = r1 < 0 ? 0 : r1;
    c.ah_src = x + ((size_t)r0 * D_ + chh * 8) * 2;
    c.ah_src1 = x + ((size_t)r1 * D_ + chh * 8) * 2;
  }
#pragma unroll
  for (int j = 0; j < 2; ++j) {
    const int off = (wid * 2 + j) * 1024 + lane * 16;
    const int k = off >> 9, rem = off & 511;
    const int seg = rem >> 8, up = (rem >> 5) & 7, h = (rem >> 4) & 1;
    const int f = (k & 3) | (((k >> 3) & 1) << 2);
    int U = seg * 8 + (up ^ f);
    U = U > 14 ? 14 : U;
    const int sec = U / 5, uu = U % 5;
    c.b_src[j] = wqkv + ((size_t)k * (3 * D_) + sec * D_ + hg * 80 + uu * 16 + h * 8) * 2;
  }
  const int g = lane >> 4, q = (lane >> 2) & 3, p = lane & 3, fr = lane & 15;
  const int arow0 = wm ? 80 : 0;
  c.a_rd = (arow0 + fr) * 128 + ((g ^ (lane & 7)) << 4);
#pragma unroll
  for (int n = 0; n < 4; ++n) {
    const int U = wn * 4 + n;
    const int f = q | ((g & 1) << 2);
    c.b_rd[n] = (8 * g + q) * 512 + (U >> 3) * 256 + (((U & 7) ^ f) << 5) + p * 8;
  }
  STAMP(oh + (64u << 20), 1);
  qkv_issue<-4>(smem, c);
  qkv_issue<-3>(smem, c);
  qkv_issue<-2>(smem, c);
  qkv_issue<-1>(smem, c);
  pack_store(pk, ws);
  if (wm == 0) qkv_main<5>(smem, c, lane, arow0, wn);
  else qkv_main<4>(smem, c, lane, arow0, wn);
  __syncthreads();
  STAMP(oh + (64u << 20), 2);

  const int hh = tid >> 7, r = tid & 127;
  const int s = s0 + r;
  const char* qp = smem + (16 + r) * QE_STRIDE + hh * 40;
  unsigned qv[10];
#pragma unroll
  for (int i = 0; i < 5; ++i) {
    u32x2 t = *(const u32x2*)(qp + i * 8);
    qv[2 * i] = t[0]; qv[2 * i + 1] = t[1];
  }
  float lg[8];
  float mx = -3.0e38f;
#pragma unroll
  for (int w = 0; w < 8; ++w) {
    const char* kp = qp + 160 - 2 * w * QE_STRIDE;
    float d = 0.f;
#pragma unroll
    for (int i = 0; i < 5; ++i) {
      u32x2 t = *(const u32x2*)(kp + i * 8);
      d = dot2(qv[2 * i], t[0], d);
      d = dot2(qv[2 * i + 1], t[1], d);
    }
    float l = rbf(rbf(d) * 0.22360679774997896f);
    l = (s - 2 * w >= 0) ? l : rbf(-1e9f);
    lg[w] = l;
    mx = fmaxf(mx, l);
  }
  float sum = 0.f;
#pragma unroll
  for (int w = 0; w < 8; ++w) { lg[w] = __expf(lg[w] - mx); sum += lg[w]; }
  const float inv = 1.0f / sum;
  f32x2 o[10];
#pragma unroll
  for (int i = 0; i < 10; ++i) o[i] = f32x2{0.f, 0.f};
#pragma unroll
  for (int w = 0; w < 8; ++w) {
    const float pw = rbf(lg[w] * inv);
    const char* vp = qp + 320 - 2 * w * QE_STRIDE;
#pragma unroll
    for (int i = 0; i < 5; ++i) {
      u32x2 t = *(const u32x2*)(vp + i * 8);
      const unsigned t0 = t[0], t1 = t[1];
      o[2 * i] += pw * f32x2{bflo(t0), bfhi(t0)};
      o[2 * i + 1] += pw * f32x2{bflo(t1), bfhi(t1)};
    }
  }
#pragma unroll
  for (int i = 0; i < 5; ++i) {
    u32x2 t;
    t[0] = pack2(o[2 * i][0], o[2 * i][1]);
    t[1] = pack2(o[2 * i + 1][0], o[2 * i + 1][1]);
    *(u32x2*)(smem + (16 + r) * QE_STRIDE + hh * 40 + i * 8) = t;
  }
  __syncthreads();
  const rsrc_t ro = mk_rsrc(oh, S_ * KOH * 2);
#pragma unroll
  for (int i = 0; i < 5; ++i) {
    const int idx = tid + 512 * i;
    const int rr = idx / 20, part = idx % 20;
    u32x2 t = *(const u32x2*)(smem + (16 + rr) * QE_STRIDE + part * 8);
    st8(ro, ((s0 + rr) * KOH + hg * 80) * 2 + part * 8, t);
  }
}

__global__ __launch_bounds__(512, 2) void k_gemm1(const char* x, const char* __restrict__ wqkv,
                                                  const char* __restrict__ wo, const char* __restrict__ w1,
                                                  const char* __restrict__ w3, const char* __restrict__ w2,
                                                  char* ws) {
  __shared__ __attribute__((aligned(16))) char smem[G1_LDS];
  STAMP(ws + WS_OH + (64u << 20), 0);
  const int b = blockIdx.x;
  const int xc = b & 7, idx = b >> 3;
  const int rh = xc & 1, j = xc >> 1;
  const int i2 = idx & 15;
  const int rt = rh * 8 + (i2 >> 1), cs = 2 * j + (i2 & 1);
  if (idx < 16) gu_block(smem, rt, cs, x, w1, w3, wo, w2, ws);
  else qkv_block(smem, rt, cs, x, wqkv, wo, w2, ws);
  STAMP(ws + WS_OH + (64u << 20), 3);
}

constexpr int G2_STAGE = 16384 + 20480;
constexpr int G2_LDS = 4 * G2_STAGE;
constexpr int G2_NS = KOH / 128;

struct G2Ctx {
  const char* a_src;
  const char* b_src;
  int a_rd, b_rd;
  int wid;
};
struct G2Frag { bf16x8 a[2]; bf16x4 bl[5], bh[5], cl[5], ch[5]; };

template <int C>
__device__ __forceinline__ void g2_issue(char* smem, const G2Ctx& c) {
  if constexpr (C < G2_NS) {
    char* dst = smem + (C & 3) * G2_STAGE;
#pragma unroll
    for (int j = 0; j < 4; ++j)
      glds16(c.a_src + (size_t)(j & 1) * 8 * KOH * 2 + C * 256 + (j >> 1) * 128,
             dst + (j >> 1) * 8192 + (c.wid * 2 + (j & 1)) * 1024);
#pragma unroll
    for (int j = 0; j < 5; ++j) glds16(c.b_src + C * 20480 + j * 1024, dst + 16384 + c.wid * 5120 + j * 1024);
  }
}
template <int C>
__device__ __forceinline__ void g2_load_step(char* smem, const G2Ctx& c) {
  constexpr int ahead = (G2_NS - 1 - C) < 2 ? (G2_NS - 1 - C) : 2;
  wait_vm<9 * ahead>();
  __builtin_amdgcn_s_barrier();
  g2_issue<C + 3>(smem, c);
  if constexpr (C + 1 < G2_NS) g2_load_step<C + 1>(smem, c);
}
template <int C>
__device__ __forceinline__ void g2_mma(f32x4 (&accA)[5], f32x4 (&accB)[5], G2Frag& prev) {
#pragma unroll
  for (int n = 0; n < 5; ++n) {
    if constexpr (C < 5)
      accA[n] = __builtin_amdgcn_mfma_f32_16x16x32_bf16(cat8(prev.bl[n], prev.bh[n]), prev.a[0], accA[n], 0, 0, 0);
    else
      accB[n] = __builtin_amdgcn_mfma_f32_16x16x32_bf16(cat8(prev.bl[n], prev.bh[n]), prev.a[0], accB[n], 0, 0, 0);
  }
#pragma unroll
  for (int n = 0; n < 5; ++n) {
    if constexpr (C < 5)
      accA[n] = __builtin_amdgcn_mfma_f32_16x16x32_bf16(cat8(prev.cl[n], prev.ch[n]), prev.a[1], accA[n], 0, 0, 0);
    else
      accB[n] = __builtin_amdgcn_mfma_f32_16x16x32_bf16(cat8(prev.cl[n], prev.ch[n]), prev.a[1], accB[n], 0, 0, 0);
  }
}
template <int C>
__device__ __forceinline__ void g2_step(unsigned lbase, const G2Ctx& c, f32x4 (&accA)[5], f32x4 (&accB)[5],
                                        G2Frag& prev, G2Frag& cur) {
  __builtin_amdgcn_sched_barrier(0);
  if constexpr (C > 0) {
    lgkm_pin(prev.bl, prev.bh);
    lgkm_pin(prev.cl, prev.ch);
    asm volatile("" : "+v"(prev.a[0]), "+v"(prev.a[1]));
  }
  if constexpr (C < G2_NS) {
    __builtin_amdgcn_s_barrier();
    const unsigned sb = lbase + (C & 3) * G2_STAGE;
    rd128_issue<0>(cur.a[0], sb + c.a_rd);
    rd128_issue<0>(cur.a[1], sb + (c.a_rd ^ 64));
    const unsigned ba = sb + c.b_rd;
    tr_issue<0>(cur.bl[0], ba);    tr_issue<256>(cur.bh[0], ba);
    tr_issue<512>(cur.bl[1], ba);  tr_issue<512 + 256>(cur.bh[1], ba);
    tr_issue<1024>(cur.bl[2], ba); tr_issue<1024 + 256>(cur.bh[2], ba);
    tr_issue<1536>(cur.bl[3], ba); tr_issue<1536 + 256>(cur.bh[3], ba);
    tr_issue<2048>(cur.bl[4], ba); tr_issue<2048 + 256>(cur.bh[4], ba);
    tr_issue<5120>(cur.cl[0], ba);        tr_issue<5120 + 256>(cur.ch[0], ba);
    tr_issue<5120 + 512>(cur.cl[1], ba);  tr_issue<5120 + 512 + 256>(cur.ch[1], ba);
    tr_issue<5120 + 1024>(cur.cl[2], ba); tr_issue<5120 + 1024 + 256>(cur.ch[2], ba);
    tr_issue<5120 + 1536>(cur.cl[3], ba); tr_issue<5120 + 1536 + 256>(cur.ch[3], ba);
    tr_issue<5120 + 2048>(cur.cl[4], ba); tr_issue<5120 + 2048 + 256>(cur.ch[4], ba);
    __builtin_amdgcn_sched_barrier(0);
  }
  if constexpr (C > 0) g2_mma<C - 1>(accA, accB, prev);
  if constexpr (C < G2_NS) g2_step<C + 1>(lbase, c, accA, accB, cur, prev);
}

__global__ __launch_bounds__(768, 3) void k_gemm2(const char* __restrict__ ws, const char* __restrict__ xin,
                                                  char* __restrict__ out) {
  __shared__ __attribute__((aligned(16))) char smem[G2_LDS];
  const char* oh = ws + WS_OH;
  const int tid = threadIdx.x, lane = tid & 63;
  const int wid = __builtin_amdgcn_readfirstlane(tid >> 6);
  const int b = blockIdx.x;
  const int x = b & 7, idx = b >> 3;
  const int rt = (x >> 1) * 8 + (idx >> 2), ct = (x & 1) * 4 + (idx & 3);
  const int row0 = rt * 64, col0 = ct * 80;
  const int kk = wid >> 2, mq = wid & 3;
  STAMP((char*)oh + (65u << 20), 0);
  G2Ctx c;
  if (wid >= 8) {
    c.wid = wid - 8;
    const int r = lane >> 3, ch = (lane & 7) ^ r;
    c.a_src = oh + ((size_t)(row0 + c.wid * 16 + r) * KOH + ch * 8) * 2;
    c.b_src = ws + WS_P2 + (size_t)ct * 34 * 10240 + c.wid * 5120 + lane * 16;
    g2_issue<0>(smem, c); g2_issue<1>(smem, c); g2_issue<2>(smem, c);
    g2_load_step<0>(smem, c);
    __builtin_amdgcn_s_barrier(); __builtin_amdgcn_s_barrier();
    return;
  }
  c.wid = wid;
  const int g = lane >> 4, q = (lane >> 2) & 3, p = lane & 3, fr = lane & 15;
  c.a_rd = kk * 8192 + (mq * 16 + fr) * 128 + ((g ^ (lane & 7)) << 4);
  c.b_rd = 16384 + kk * 10240 + ((g >> 1) * 20 + (g & 1)) * 128 + q * 32 + p * 8;

  f32x4 accA[5], accB[5];
#pragma unroll
  for (int n = 0; n < 5; ++n) { accA[n] = f32x4{0.f, 0.f, 0.f, 0.f}; accB[n] = accA[n]; }
  const size_t go = ((size_t)(row0 + mq * 16 + fr) * D_ + col0 + g * 4) * 2;
  u32x2 xv[3];
#pragma unroll
  for (int n = 0; n < 3; ++n) xv[n] = *(const u32x2*)(xin + go + (kk ? (n < 2 ? n + 3 : 4) : n) * 32);
  STAMP((char*)oh + (65u << 20), 1);
  {
    G2Frag f0_, f1_;
    g2_step<0>((unsigned)(size_t)smem, c, accA, accB, f0_, f1_);
  }
  STAMP((char*)oh + (65u << 20), 2);
  __builtin_amdgcn_s_barrier();
  f32x4* ex = (f32x4*)smem + wid * 6 * 64 + lane;
  if (kk == 0) {
#pragma unroll
    for (int n = 0; n < 2; ++n) { ex[n * 64] = accA[n + 3]; ex[(3 + n) * 64] = accB[n + 3]; }
  } else {
#pragma unroll
    for (int n = 0; n < 3; ++n) { ex[n * 64] = accA[n]; ex[(3 + n) * 64] = accB[n]; }
  }
  __syncthreads();
  const f32x4* pe = (const f32x4*)smem + (wid ^ 4) * 6 * 64 + lane;
  auto fin = [&](f32x4 a, f32x4 bq, u32x2 xx, int n) {
    const unsigned x0 = xx[0], x1 = xx[1];
    const float xs[4] = {bflo(x0), bfhi(x0), bflo(x1), bfhi(x1)};
    float r[4];
#pragma unroll
    for (int e = 0; e < 4; ++e) r[e] = rbf(xs[e] + rbf(a[e])) + rbf(bq[e]);
    u32x2 o;
    o[0] = pack2(r[0], r[1]);
    o[1] = pack2(r[2], r[3]);
    *(u32x2*)(out + go + n * 32) = o;
  };
  if (kk == 0) {
#pragma unroll
    for (int n = 0; n < 3; ++n) fin(accA[n] + pe[n * 64], accB[n] + pe[(3 + n) * 64], xv[n], n);
  } else {
#pragma unroll
    for (int n = 0; n < 2; ++n) fin(pe[n * 64] + accA[n + 3], pe[(3 + n) * 64] + accB[n + 3], xv[n], n + 3);
  }
  STAMP((char*)oh + (65u << 20), 3);
}

extern "C" void kernel_launch(void* const* d_in, const int* in_sizes, int n_in, void* d_out, int out_size,
                              void* d_ws, size_t ws_size, hipStream_t stream) {
  const char* x = (const char*)d_in[0];
  const char* wqkv = (const char*)d_in[1];
  const char* wo = (const char*)d_in[2];
  const char* w1 = (const char*)d_in[4];
  const char* w3 = (const char*)d_in[5];
  const char* w2 = (const char*)d_in[6];
  char* ws = (char*)d_ws;
  k_ln<<<S_ / LN_W, 64 * LN_W, 0, stream>>>((const unsigned*)x, (unsigned*)ws);
  k_gemm1<<<256, 512, 0, stream>>>(ws, wqkv, wo, w1, w3, w2, ws);
  k_gemm2<<<256, 768, 0, stream>>>(ws, x, (char*)d_out);
}
```

```cpp
#include <hip/hip_runtime.h>
#include <hip/hip_bf16.h>
#include <stdint.h>
#include <type_traits>

typedef __attribute__((ext_vector_type(8))) short bf16x8;
typedef __attribute__((ext_vector_type(4))) short bf16x4;
typedef __attribute__((ext_vector_type(4))) float f32x4;
typedef __attribute__((ext_vector_type(2))) float f32x2;
typedef __attribute__((ext_vector_type(2))) __bf16 bf2;
typedef __attribute__((ext_vector_type(2))) unsigned u32x2;
typedef __attribute__((ext_vector_type(4))) unsigned u32x4;

constexpr int S_ = 2048, D_ = 640, F_ = 1536;
constexpr int KOH = D_ + F_;
constexpr unsigned WS_OH = 4u << 20, WS_P2 = 24u << 20;

__device__ __forceinline__ float rbf(float f) { return (float)(__bf16)f; }
__device__ __forceinline__ unsigned pack2(float lo, float hi) {
  bf2 v; v[0] = (__bf16)lo; v[1] = (__bf16)hi;
  return __builtin_bit_cast(unsigned, v);
}
__device__ __forceinline__ float bflo(unsigned u) { return __builtin_bit_cast(float, u << 16); }
__device__ __forceinline__ float bfhi(unsigned u) { return __builtin_bit_cast(float, u & 0xffff0000u); }
__device__ __forceinline__ f32x2 rbf2(f32x2 v) {
  const unsigned u = pack2(v[0], v[1]);
  return f32x2{bflo(u), bfhi(u)};
}
__device__ __forceinline__ float dot2(unsigned a, unsigned b, float c) {
  return __builtin_amdgcn_fdot2_f32_bf16(__builtin_bit_cast(bf2, a), __builtin_bit_cast(bf2, b), c, false);
}
constexpr int ST_AUX = 16;
typedef __amdgpu_buffer_rsrc_t rsrc_t;
__device__ __forceinline__ rsrc_t mk_rsrc(void* p, unsigned bytes) {
  return __builtin_amdgcn_make_buffer_rsrc(p, 0, bytes, 0x00020000);
}
__device__ __forceinline__ void st16(rsrc_t r, unsigned off, u32x4 v) {
  __builtin_amdgcn_raw_buffer_store_b128(v, r, off, 0, ST_AUX);
}
__device__ __forceinline__ void st8(rsrc_t r, unsigned off, u32x2 v) {
  __builtin_amdgcn_raw_buffer_store_b64(v, r, off, 0, ST_AUX);
}
template <int N> __device__ __forceinline__ void wait_vm() {
  asm volatile("s_waitcnt vmcnt(%0)" ::"i"(N) : "memory");
}
__device__ __forceinline__ void glds16(const void* g, char* l) {
  __builtin_amdgcn_global_load_lds((const unsigned*)g, (unsigned*)l, 16, 0, 0);
}
__device__ __forceinline__ void glds16_lo(const void* g, unsigned lds_addr, unsigned long long mask) {
  unsigned keep;
  asm volatile(
      "s_mov_b32 %0, m0\n\ts_mov_b32 m0, %2\n\ts_mov_b64 exec, %3\n\tglobal_load_lds_dwordx4 %1, off\n\t"
      "s_mov_b64 exec, -1\n\ts_mov_b32 m0, %0"
      : "=&s"(keep) : "v"(g), "s"(lds_addr), "s"(mask) : "memory");
}
template <int IMM>
__device__ __forceinline__ void tr_issue(bf16x4& d, unsigned addr) {
  asm volatile("ds_read_b64_tr_b16 %0, %1 offset:%2" : "=v"(d) : "v"(addr), "i"(IMM) : "memory");
}
template <int IMM>
__device__ __forceinline__ void rd128_issue(bf16x8& d, unsigned addr) {
  asm volatile("ds_read_b128 %0, %1 offset:%2" : "=v"(d) : "v"(addr), "i"(IMM) : "memory");
}
__device__ __forceinline__ void pin(bf16x8 (&a)[4]) {
  asm volatile("" : "+v"(a[0]), "+v"(a[1]), "+v"(a[2]), "+v"(a[3]));
}
__device__ __forceinline__ void pin(bf16x8 (&a)[5]) {
  asm volatile("" : "+v"(a[0]), "+v"(a[1]), "+v"(a[2]), "+v"(a[3]), "+v"(a[4]));
}
__device__ __forceinline__ void lgkm_pin(bf16x4 (&l)[4], bf16x4 (&h)[4]) {
  asm volatile("s_waitcnt lgkmcnt(0)"
               : "+v"(l[0]), "+v"(l[1]), "+v"(l[2]), "+v"(l[3]), "+v"(h[0]), "+v"(h[1]), "+v"(h[2]), "+v"(h[3])
               :: "memory");
}
__device__ __forceinline__ void lgkm_pin(bf16x4 (&l)[5], bf16x4 (&h)[5]) {
  asm volatile("s_waitcnt lgkmcnt(0)"
               : "+v"(l[0]), "+v"(l[1]), "+v"(l[2]), "+v"(l[3]), "+v"(l[4]), "+v"(h[0]), "+v"(h[1]), "+v"(h[2]),
                 "+v"(h[3]), "+v"(h[4])
               :: "memory");
}
__device__ __forceinline__ void lgkm_pin(bf16x4 (&l)[6], bf16x4 (&h)[6]) {
  asm volatile("s_waitcnt lgkmcnt(0)"
               : "+v"(l[0]), "+v"(l[1]), "+v"(l[2]), "+v"(l[3]), "+v"(l[4]), "+v"(l[5]), "+v"(h[0]), "+v"(h[1]),
                 "+v"(h[2]), "+v"(h[3]), "+v"(h[4]), "+v"(h[5])
               :: "memory");
}
__device__ __forceinline__ bf16x8 cat8(bf16x4 a, bf16x4 b) {
  return __builtin_shufflevector(a, b, 0, 1, 2, 3, 4, 5, 6, 7);
}

constexpr int LN_W = 4;
__global__ __launch_bounds__(64 * LN_W) void k_ln(const unsigned* __restrict__ x, unsigned* __restrict__ xn) {
  const int lane = threadIdx.x & 63;
  const int row = blockIdx.x * LN_W + (threadIdx.x >> 6);
  const unsigned* xr = x + (size_t)row * (D_ / 2);
  unsigned v[5];
#pragma unroll
  for (int i = 0; i < 5; ++i) v[i] = xr[lane + 64 * i];
  float a[10];
  float s = 0.f;
#pragma unroll
  for (int i = 0; i < 5; ++i) {
    a[2 * i] = bflo(v[i]); a[2 * i + 1] = bfhi(v[i]);
    s += a[2 * i] + a[2 * i + 1];
  }
#pragma unroll
  for (int o = 32; o > 0; o >>= 1) s += __shfl_xor(s, o);
  const float mean = s * (1.0f / D_);
  float q = 0.f;
#pragma unroll
  for (int i = 0; i < 10; ++i) { float d = a[i] - mean; q += d * d; }
#pragma unroll
  for (int o = 32; o > 0; o >>= 1) q += __shfl_xor(q, o);
  const float var = q * (1.0f / D_);
  const float mb = rbf(mean);
  const float sd = rbf(sqrtf(rbf(rbf(var) + 1e-5f)));
  unsigned* o = xn + (size_t)row * (D_ / 2);
#pragma unroll
  for (int i = 0; i < 5; ++i) {
    float l = rbf(a[2 * i] - mb) / sd;
    float h = rbf(a[2 * i + 1] - mb) / sd;
    o[lane + 64 * i] = pack2(l, h);
  }
}

constexpr int N_P2 = 8 * 34 * 640;
__device__ __forceinline__ const char* p2_src(int i, const char* wo, const char* w2) {
  const int ct = i / (34 * 640), cc = (i / 640) % 34, j = i % 640;
  const int kk = j / 320, jj = j % 320;
  const int bi = jj >> 3, q = (jj >> 1) & 3, h = jj & 1;
  const int ghi = bi / 20, U = (bi % 20) >> 2, pos = bi & 3;
  const int k = 64 * cc + 32 * kk + 4 * (4 * ghi + 2 * (pos & 1) + (pos >> 1)) + q;
  return (k < D_ ? wo + (size_t)k * D_ * 2 : w2 + (size_t)(k - D_) * D_ * 2) + (80 * ct + 16 * U + 8 * h) * 2;
}
struct PackRegs { u32x4 v0, v1; };
__device__ __forceinline__ void pack_load(PackRegs& p, const char* wo, const char* w2) {
  const int i0 = blockIdx.x * 680 + threadIdx.x;
  p.v0 = *(const u32x4*)p2_src(i0, wo, w2);
  p.v1 = *(const u32x4*)p2_src(i0 + (threadIdx.x < 168 ? 512 : 0), wo, w2);
}
__device__ __forceinline__ void pack_store(const PackRegs& p, char* ws) {
  const unsigned i0 = blockIdx.x * 680 + threadIdx.x;
  *(u32x4*)(ws + WS_P2 + (size_t)i0 * 16) = p.v0;
  *(u32x4*)(ws + WS_P2 + (size_t)(i0 + (threadIdx.x < 168 ? 512 : 0)) * 16) = p.v1;
}

constexpr int G1_LDS = 147456;

struct GuCtx {
  const char* a_src;
  const char* b_src[3];
  int a_rd;
  int b_rd[6];
  int wid;
};

template <int T, int J>
__device__ __forceinline__ void gu_issue1(char* smem, const GuCtx& c) {
  constexpr int ST = (T + 4) / 2;
  constexpr int HF = (T + 4) & 1;
  constexpr int BT = T + 3;
  if constexpr (J == 0) {
    if constexpr (ST < 10)
      glds16(c.a_src + (size_t)HF * 64 * D_ * 2 + ST * 128, smem + (ST % 3) * 16384 + (HF * 64 + c.wid * 8) * 128);
  } else {
    if constexpr (BT >= 0 && BT < 20)
      glds16(c.b_src[J - 1] + (size_t)BT * 32 * F_ * 2, smem + 49152 + (BT & 3) * 24576 + (c.wid * 3 + J - 1) * 1024);
  }
}
template <int T>
__device__ __forceinline__ void gu_issue(char* smem, const GuCtx& c) {
  gu_issue1<T, 0>(smem, c); gu_issue1<T, 1>(smem, c); gu_issue1<T, 2>(smem, c); gu_issue1<T, 3>(smem, c);
}
template <int T> constexpr int gu_gsize() {
  return (T < -4) ? 0 : (((T + 4) / 2 < 10) ? 1 : 0) + ((T + 3 >= 0 && T + 3 < 20) ? 3 : 0);
}

struct GuFrag { bf16x8 a[4]; bf16x4 bl[6], bh[6]; };

template <int T, int I0>
__device__ __forceinline__ void gu_side(char* smem, unsigned ab, unsigned bb, const GuCtx& c, GuFrag& cur) {
  if constexpr (T < 20) {
    if constexpr (I0 % 6 == 3) gu_issue1<T, I0 / 6>(smem, c);
    else {
      constexpr int I = I0 - (I0 + 2) / 6;
      if constexpr (I < 4) rd128_issue<((T / 2) % 3) * 16384 + I * 2048>(cur.a[I], ab);
      else if constexpr (I < 16) {
        constexpr int n = (I - 4) >> 1;
        if constexpr (((I - 4) & 1) == 0) tr_issue<(T & 1) * 24576>(cur.bl[n], bb + c.b_rd[n]);
        else tr_issue<(T & 1) * 24576 + 3072>(cur.bh[n], bb + c.b_rd[n]);
      }
    }
  }
}
template <int T, int I>
__device__ __forceinline__ void gu_slots(char* smem, unsigned ab, unsigned bb, const GuCtx& c, f32x4 (&acc)[4][6],
                                         GuFrag& prev, GuFrag& cur) {
  if constexpr (T > 0) {
    constexpr int m = I / 6, n = I % 6;
    acc[m][n] = __builtin_amdgcn_mfma_f32_16x16x32_bf16(cat8(prev.bl[n], prev.bh[n]), prev.a[m], acc[m][n], 0, 0, 0);
  }
  gu_side<T, I>(smem, ab, bb, c, cur);
  __builtin_amdgcn_sched_barrier(0);
  if constexpr (I + 1 < 24) gu_slots<T, I + 1>(smem, ab, bb, c, acc, prev, cur);
}

template <int T>
__device__ __forceinline__ void gu_step(char* smem, unsigned lbase, const GuCtx& c, f32x4 (&acc)[4][6],
                                        GuFrag& prev, GuFrag& cur) {
  __builtin_amdgcn_sched_barrier(0);
  if constexpr (T > 0) { lgkm_pin(prev.bl, prev.bh); pin(prev.a); }
  if constexpr (T < 20) {
    wait_vm<gu_gsize<T - 2>() + gu_gsize<T - 1>()>();
    __builtin_amdgcn_s_barrier();
  }
  const unsigned bb = lbase + 49152 + ((T & 3) >> 1) * 49152;
  const unsigned ab = lbase + ((T & 1) ? (c.a_rd ^ 64) : c.a_rd);
  gu_slots<T, 0>(smem, ab, bb, c, acc, prev, cur);
  if constexpr (T < 20) gu_step<T + 1>(smem, lbase, c, acc, cur, prev);
}

__device__ __forceinline__ void gu_block(char* smem, int rt, int slice, const char* x, const char* w1,
                                         const char* w3, const char* wo, const char* w2, char* ws) {
  const int tid = threadIdx.x, lane = tid & 63;
  const int wid = __builtin_amdgcn_readfirstlane(tid >> 6);
  const int wm = wid >> 2, wn = wid & 3;
  const int row0 = rt * 128, f0 = slice * 192;
  char* oh = ws + WS_OH;
  PackRegs pk;
  pack_load(pk, wo, w2);
  GuCtx c;
  c.wid = wid;
  {
    const int r = lane >> 3, ch = (lane & 7) ^ r;
    c.a_src = x + ((size_t)(row0 + wid * 8 + r) * D_ + ch * 8) * 2;
  }
#pragma unroll
  for (int j = 0; j < 3; ++j) {
    const int off = (wid * 3 + j) * 1024 + lane * 16;
    const int k = off / 768, rem = off % 768;
    const int seg = rem >> 8, up = (rem >> 5) & 7, h = (rem >> 4) & 1;
    const int f = (k & 3) | (((k >> 3) & 1) << 2);
    const int col = (seg * 8 + (up ^ f)) * 16 + h * 8;
    const char* w = col < 192 ? w1 : w3;
    const int cc = col < 192 ? col : col - 192;
    c.b_src[j] = w + ((size_t)k * F_ + f0 + cc) * 2;
  }
  const int g = lane >> 4, q = (lane >> 2) & 3, p = lane & 3, fr = lane & 15;
  c.a_rd = (wm * 64 + fr) * 128 + ((g ^ (lane & 7)) << 4);
#pragma unroll
  for (int n = 0; n < 6; ++n) {
    const int U = (n < 3) ? 3 * wn + n : 12 + 3 * wn + (n - 3);
    const int f = q | ((g & 1) << 2);
    c.b_rd[n] = (8 * g + q) * 768 + (U >> 3) * 256 + (((U & 7) ^ f) << 5) + p * 8;
  }
  f32x4 acc[4][6];
#pragma unroll
  for (int m = 0; m < 4; ++m)
#pragma unroll
    for (int n = 0; n < 6; ++n) acc[m][n] = f32x4{0.f, 0.f, 0.f, 0.f};
  gu_issue<-4>(smem, c);
  gu_issue<-3>(smem, c);
  gu_issue<-2>(smem, c);
  gu_issue<-1>(smem, c);
  pack_store(pk, ws);
  {
    GuFrag f0_, f1_;
    gu_step<0>(smem, (unsigned)(size_t)smem, c, acc, f0_, f1_);
  }
  __builtin_amdgcn_s_barrier();
  const rsrc_t ro = mk_rsrc(oh, S_ * KOH * 2);
#pragma unroll
  for (int m = 0; m < 4; ++m) {
#pragma unroll
    for (int n = 0; n < 3; ++n) {
      u32x2 o;
#pragma unroll
      for (int r = 0; r < 2; ++r) {
        const f32x2 gt = rbf2(f32x2{acc[m][n][2 * r], acc[m][n][2 * r + 1]});
        const f32x2 up = rbf2(f32x2{acc[m][n + 3][2 * r], acc[m][n + 3][2 * r + 1]});
        const f32x2 t = gt * -1.4426950408889634f;
        f32x2 e = {__builtin_amdgcn_exp2f(t[0]), __builtin_amdgcn_exp2f(t[1])};
        e += 1.0f;
        const f32x2 rc = {__builtin_amdgcn_rcpf(e[0]), __builtin_amdgcn_rcpf(e[1])};
        const f32x2 hv = rbf2(gt * rc) * up;
        o[r] = pack2(hv[0], hv[1]);
      }
      *(u32x2*)(smem + (wm * 64 + m * 16 + fr) * 400 + (wn * 48 + n * 16 + g * 4) * 2) = o;
    }
  }
  __syncthreads();
#pragma unroll
  for (int i = 0; i < 6; ++i) {
    const int idx = tid + 512 * i;
    const int rr = idx / 24, ch = idx % 24;
    const u32x4 v = *(const u32x4*)(smem + rr * 400 + ch * 16);
    st16(ro, ((row0 + rr) * KOH + D_ + f0) * 2 + ch * 16, v);
  }
}

struct QkvCtx {
  const char* a_src;
  const char* ah_src;
  const char* ah_src1;
  const char* b_src[2];
  int a_rd;
  int b_rd[4];
  int wid;
};
constexpr int QA_STAGE = 144 * 128;
constexpr int QB_BASE = 3 * QA_STAGE;
constexpr int QB_SLOT = 32 * 512;

template <int T, int J>
__device__ __forceinline__ void qkv_issue1(char* smem, const QkvCtx& c) {
  constexpr int ST = (T + 4) / 2;
  constexpr int HF = (T + 4) & 1;
  constexpr int BT = T + 3;
  if constexpr (J == 0) {
    if constexpr (ST < 10)
      glds16(c.a_src + (size_t)HF * 64 * D_ * 2 + ST * 128,
             smem + (ST % 3) * QA_STAGE + (16 + HF * 64 + c.wid * 8) * 128);
  } else if constexpr (J == 1) {
    if constexpr (ST < 10)
      glds16_lo((HF ? c.ah_src1 : c.ah_src) + ST * 128,
                (unsigned)(size_t)smem + (ST % 3) * QA_STAGE + (HF * 8 + c.wid) * 128, 0xffull);
  } else {
    if constexpr (BT >= 0 && BT < 20)
      glds16(c.b_src[J - 2] + (size_t)BT * 32 * (3 * D_) * 2,
             smem + QB_BASE + (BT & 3) * QB_SLOT + (c.wid * 2 + J - 2) * 1024);
  }
}
template <int T>
__device__ __forceinline__ void qkv_issue(char* smem, const QkvCtx& c) {
  qkv_issue1<T, 0>(smem, c); qkv_issue1<T, 1>(smem, c); qkv_issue1<T, 2>(smem, c); qkv_issue1<T, 3>(smem, c);
}
template <int T> constexpr int qkv_gsize() {
  return (T < -4) ? 0 : (((T + 4) / 2 < 10) ? 2 : 0) + ((T + 3 >= 0 && T + 3 < 20) ? 2 : 0);
}

template <int MT> struct QkvFrag { bf16x8 a[MT]; bf16x4 bl[4], bh[4]; };

template <int T, int MT, int I0>
__device__ __forceinline__ void qkv_side(char* smem, unsigned ab, unsigned bb, const QkvCtx& c, QkvFrag<MT>& cur) {
  if constexpr (T < 20) {
    if constexpr (I0 % MT == 2) qkv_issue1<T, I0 / MT>(smem, c);
    else {
      constexpr int I = I0 - (I0 + MT - 3) / MT;
      if constexpr (I < MT) rd128_issue<((T / 2) % 3) * QA_STAGE + I * 2048>(cur.a[I], ab);
      else if constexpr (I < MT + 8) {
        constexpr int n = (I - MT) >> 1;
        if constexpr (((I - MT) & 1) == 0) tr_issue<(T & 1) * QB_SLOT>(cur.bl[n], bb + c.b_rd[n]);
        else tr_issue<(T & 1) * QB_SLOT + 2048>(cur.bh[n], bb + c.b_rd[n]);
      }
    }
  }
}
template <int T, int MT, int I>
__device__ __forceinline__ void qkv_slots(char* smem, unsigned ab, unsigned bb, const QkvCtx& c, f32x4 (&acc)[MT][4],
                                          QkvFrag<MT>& prev, QkvFrag<MT>& cur) {
  if constexpr (T > 0) {
    constexpr int m = I / 4, n = I % 4;
    acc[m][n] = __builtin_amdgcn_mfma_f32_16x16x32_bf16(cat8(prev.bl[n], prev.bh[n]), prev.a[m], acc[m][n], 0, 0, 0);
  }
  qkv_side<T, MT, I>(smem, ab, bb, c, cur);
  __builtin_amdgcn_sched_barrier(0);
  if constexpr (I + 1 < MT * 4) qkv_slots<T, MT, I + 1>(smem, ab, bb, c, acc, prev, cur);
}

template <int T, int MT>
__device__ __forceinline__ void qkv_step(char* smem, unsigned lbase, const QkvCtx& c, f32x4 (&acc)[MT][4],
                                         QkvFrag<MT>& prev, QkvFrag<MT>& cur) {
  __builtin_amdgcn_sched_barrier(0);
  if constexpr (T > 0) { lgkm_pin(prev.bl, prev.bh); pin(prev.a); }
  if constexpr (T < 20) {
    wait_vm<qkv_gsize<T - 2>() + qkv_gsize<T - 1>()>();
    __builtin_amdgcn_s_barrier();
  }
  const unsigned ab = lbase + ((T & 1) ? (c.a_rd ^ 64) : c.a_rd);
  const unsigned bb = lbase + QB_BASE + ((T & 3) >> 1) * 2 * QB_SLOT;
  qkv_slots<T, MT, 0>(smem, ab, bb, c, acc, prev, cur);
  if constexpr (T < 20) qkv_step<T + 1, MT>(smem, lbase, c, acc, cur, prev);
}

constexpr int QE_STRIDE = 488;

template <int MT>
__device__ __forceinline__ void qkv_main(char* smem, const QkvCtx& c, int lane, int arow0, int wn) {
  f32x4 acc[MT][4];
#pragma unroll
  for (int m = 0; m < MT; ++m)
#pragma unroll
    for (int n = 0; n < 4; ++n) acc[m][n] = f32x4{0.f, 0.f, 0.f, 0.f};
  {
    QkvFrag<MT> f0_, f1_;
    qkv_step<0, MT>(smem, (unsigned)(size_t)smem, c, acc, f0_, f1_);
  }
  __builtin_amdgcn_s_barrier();
  const int g = lane >> 4, fr = lane & 15;
#pragma unroll
  for (int n = 0; n < 4; ++n) {
    const int U = wn * 4 + n;
    if (U < 15) {
#pragma unroll
      for (int m = 0; m < MT; ++m) {
        u32x2 o;
        o[0] = pack2(acc[m][n][0], acc[m][n][1]);
        o[1] = pack2(acc[m][n][2], acc[m][n][3]);
        *(u32x2*)(smem + (arow0 + m * 16 + fr) * QE_STRIDE + U * 32 + g * 8) = o;
      }
    }
  }
}

__device__ __forceinline__ void qkv_block(char* smem, int rt, int hg, const char* x, const char* wqkv,
                                          const char* wo, const char* w2, char* ws) {
  const int tid = threadIdx.x, lane = tid & 63;
  const int wid = __builtin_amdgcn_readfirstlane(tid >> 6);
  const int wm = wid >> 2, wn = wid & 3;
  const int s0 = rt * 128;
  char* oh = ws + WS_OH;
  PackRegs pk;
  pack_load(pk, wo, w2);
  QkvCtx c;
  c.wid = wid;
  {
    const int r = lane >> 3, ch = (lane & 7) ^ r;
    c.a_src = x + ((size_t)(s0 + wid * 8 + r) * D_ + ch * 8) * 2;
    const int chh = (lane & 7) ^ wid;
    int r0 = s0 - 16 + wid, r1 = s0 - 8 + wid;
    r0 = r0 < 0 ? 0 : r0;
    r1 = r1 < 0 ? 0 : r1;
    c.ah_src = x + ((size_t)r0 * D_ + chh * 8) * 2;
    c.ah_src1 = x + ((size_t)r1 * D_ + chh * 8) * 2;
  }
#pragma unroll
  for (int j = 0; j < 2; ++j) {
    const int off = (wid * 2 + j) * 1024 + lane * 16;
    const int k = off >> 9, rem = off & 511;
    const int seg = rem >> 8, up = (rem >> 5) & 7, h = (rem >> 4) & 1;
    const int f = (k & 3) | (((k >> 3) & 1) << 2);
    int U = seg * 8 + (up ^ f);
    U = U > 14 ? 14 : U;
    const int sec = U / 5, uu = U % 5;
    c.b_src[j] = wqkv + ((size_t)k * (3 * D_) + sec * D_ + hg * 80 + uu * 16 + h * 8) * 2;
  }
  const int g = lane >> 4, q = (lane >> 2) & 3, p = lane & 3, fr = lane & 15;
  const int arow0 = wm ? 80 : 0;
  c.a_rd = (arow0 + fr) * 128 + ((g ^ (lane & 7)) << 4);
#pragma unroll
  for (int n = 0; n < 4; ++n) {
    const int U = wn * 4 + n;
    const int f = q | ((g & 1) << 2);
    c.b_rd[n] = (8 * g + q) * 512 + (U >> 3) * 256 + (((U & 7) ^ f) << 5) + p * 8;
  }
  qkv_issue<-4>(smem, c);
  qkv_issue<-3>(smem, c);
  qkv_issue<-2>(smem, c);
  qkv_issue<-1>(smem, c);
  pack_store(pk, ws);
  if (wm == 0) qkv_main<5>(smem, c, lane, arow0, wn);
  else qkv_main<4>(smem, c, lane, arow0, wn);
  __syncthreads();

  const int hh = tid >> 7, r = tid & 127;
  const int s = s0 + r;
  const char* qp = smem + (16 + r) * QE_STRIDE + hh * 40;
  unsigned qv[10];
#pragma unroll
  for (int i = 0; i < 5; ++i) {
    u32x2 t = *(const u32x2*)(qp + i * 8);
    qv[2 * i] = t[0]; qv[2 * i + 1] = t[1];
  }
  float lg[8];
  float mx = -3.0e38f;
#pragma unroll
  for (int w = 0; w < 8; ++w) {
    const char* kp = qp + 160 - 2 * w * QE_STRIDE;
    float d = 0.f;
#pragma unroll
    for (int i = 0; i < 5; ++i) {
      u32x2 t = *(const u32x2*)(kp + i * 8);
      d = dot2(qv[2 * i], t[0], d);
      d = dot2(qv[2 * i + 1], t[1], d);
    }
    float l = rbf(rbf(d) * 0.22360679774997896f);
    l = (s - 2 * w >= 0) ? l : rbf(-1e9f);
    lg[w] = l;
    mx = fmaxf(mx, l);
  }
  float sum = 0.f;
#pragma unroll
  for (int w = 0; w < 8; ++w) { lg[w] = __expf(lg[w] - mx); sum += lg[w]; }
  const float inv = 1.0f / sum;
  f32x2 o[10];
#pragma unroll
  for (int i = 0; i < 10; ++i) o[i] = f32x2{0.f, 0.f};
#pragma unroll
  for (int w = 0; w < 8; ++w) {
    const float pw = rbf(lg[w] * inv);
    const char* vp = qp + 320 - 2 * w * QE_STRIDE;
#pragma unroll
    for (int i = 0; i < 5; ++i) {
      u32x2 t = *(const u32x2*)(vp + i * 8);
      const unsigned t0 = t[0], t1 = t[1];
      o[2 * i] += pw * f32x2{bflo(t0), bfhi(t0)};
      o[2 * i + 1] += pw * f32x2{bflo(t1), bfhi(t1)};
    }
  }
#pragma unroll
  for (int i = 0; i < 5; ++i) {
    u32x2 t;
    t[0] = pack2(o[2 * i][0], o[2 * i][1]);
    t[1] = pack2(o[2 * i + 1][0], o[2 * i + 1][1]);
    *(u32x2*)(smem + (16 + r) * QE_STRIDE + hh * 40 + i * 8) = t;
  }
  __syncthreads();
  const rsrc_t ro = mk_rsrc(oh, S_ * KOH * 2);
#pragma unroll
  for (int i = 0; i < 5; ++i) {
    const int idx = tid + 512 * i;
    const int rr = idx / 20, part = idx % 20;
    u32x2 t = *(const u32x2*)(smem + (16 + rr) * QE_STRIDE + part * 8);
    st8(ro, ((s0 + rr) * KOH + hg * 80) * 2 + part * 8, t);
  }
}

__global__ __launch_bounds__(512, 2) void k_gemm1(const char* x, const char* __restrict__ wqkv,
                                                  const char* __restrict__ wo, const char* __restrict__ w1,
                                                  const char* __restrict__ w3, const char* __restrict__ w2,
                                                  char* ws) {
  __shared__ __attribute__((aligned(16))) char smem[G1_LDS];
  const int b = blockIdx.x;
  const int xc = b & 7, idx = b >> 3;
  const int rh = xc & 1, j = xc >> 1;
  const int i2 = idx & 15;
  const int rt = rh * 8 + (i2 >> 1), cs = 2 * j + (i2 & 1);
  if (idx < 16) gu_block(smem, rt, cs, x, w1, w3, wo, w2, ws);
  else qkv_block(smem, rt, cs, x, wqkv, wo, w2, ws);
}

constexpr int G2_STAGE = 16384 + 20480;
constexpr int G2_LDS = 4 * G2_STAGE;
constexpr int G2_NS = KOH / 128;

struct G2Ctx {
  const char* a_src;
  const char* b_src;
  int a_rd, b_rd;
  int wid;
};
struct G2Frag { bf16x8 a[2]; bf16x4 bl[5], bh[5], cl[5], ch[5]; };

template <int C>
__device__ __forceinline__ void g2_issue(char* smem, const G2Ctx& c) {
  if constexpr (C < G2_NS) {
    char* dst = smem + (C & 3) * G2_STAGE;
#pragma unroll
    for (int j = 0; j < 4; ++j)
      glds16(c.a_src + (size_t)(j & 1) * 8 * KOH * 2 + C * 256 + (j >> 1) * 128,
             dst + (j >> 1) * 8192 + (c.wid * 2 + (j & 1)) * 1024);
#pragma unroll
    for (int j = 0; j < 5; ++j) glds16(c.b_src + C * 20480 + j * 1024, dst + 16384 + c.wid * 5120 + j * 1024);
  }
}
template <int C>
__device__ __forceinline__ void g2_load_step(char* smem, const G2Ctx& c) {
  constexpr int ahead = (G2_NS - 1 - C) < 2 ? (G2_NS - 1 - C) : 2;
  wait_vm<9 * ahead>();
  __builtin_amdgcn_s_barrier();
  g2_issue<C + 3>(smem, c);
  if constexpr (C + 1 < G2_NS) g2_load_step<C + 1>(smem, c);
}
template <int C>
__device__ __forceinline__ void g2_mma(f32x4 (&accA)[5], f32x4 (&accB)[5], G2Frag& prev) {
#pragma unroll
  for (int n = 0; n < 5; ++n) {
    if constexpr (C < 5)
      accA[n] = __builtin_amdgcn_mfma_f32_16x16x32_bf16(cat8(prev.bl[n], prev.bh[n]), prev.a[0], accA[n], 0, 0, 0);
    else
      accB[n] = __builtin_amdgcn_mfma_f32_16x16x32_bf16(cat8(prev.bl[n], prev.bh[n]), prev.a[0], accB[n], 0, 0, 0);
  }
#pragma unroll
  for (int n = 0; n < 5; ++n) {
    if constexpr (C < 5)
      accA[n] = __builtin_amdgcn_mfma_f32_16x16x32_bf16(cat8(prev.cl[n], prev.ch[n]), prev.a[1], accA[n], 0, 0, 0);
    else
      accB[n] = __builtin_amdgcn_mfma_f32_16x16x32_bf16(cat8(prev.cl[n], prev.ch[n]), prev.a[1], accB[n], 0, 0, 0);
  }
}
template <int C>
__device__ __forceinline__ void g2_step(unsigned lbase, const G2Ctx& c, f32x4 (&accA)[5], f32x4 (&accB)[5],
                                        G2Frag& prev, G2Frag& cur) {
  __builtin_amdgcn_sched_barrier(0);
  if constexpr (C > 0) {
    lgkm_pin(prev.bl, prev.bh);
    lgkm_pin(prev.cl, prev.ch);
    asm volatile("" : "+v"(prev.a[0]), "+v"(prev.a[1]));
  }
  if constexpr (C < G2_NS) {
    __builtin_amdgcn_s_barrier();
    const unsigned sb = lbase + (C & 3) * G2_STAGE;
    rd128_issue<0>(cur.a[0], sb + c.a_rd);
    rd128_issue<0>(cur.a[1], sb + (c.a_rd ^ 64));
    const unsigned ba = sb + c.b_rd;
    tr_issue<0>(cur.bl[0], ba);    tr_issue<256>(cur.bh[0], ba);
    tr_issue<512>(cur.bl[1], ba);  tr_issue<512 + 256>(cur.bh[1], ba);
    tr_issue<1024>(cur.bl[2], ba); tr_issue<1024 + 256>(cur.bh[2], ba);
    tr_issue<1536>(cur.bl[3], ba); tr_issue<1536 + 256>(cur.bh[3], ba);
    tr_issue<2048>(cur.bl[4], ba); tr_issue<2048 + 256>(cur.bh[4], ba);
    tr_issue<5120>(cur.cl[0], ba);        tr_issue<5120 + 256>(cur.ch[0], ba);
    tr_issue<5120 + 512>(cur.cl[1], ba);  tr_issue<5120 + 512 + 256>(cur.ch[1], ba);
    tr_issue<5120 + 1024>(cur.cl[2], ba); tr_issue<5120 + 1024 + 256>(cur.ch[2], ba);
    tr_issue<5120 + 1536>(cur.cl[3], ba); tr_issue<5120 + 1536 + 256>(cur.ch[3], ba);
    tr_issue<5120 + 2048>(cur.cl[4], ba); tr_issue<5120 + 2048 + 256>(cur.ch[4], ba);
    __builtin_amdgcn_sched_barrier(0);
  }
  if constexpr (C > 0) g2_mma<C - 1>(accA, accB, prev);
  if constexpr (C < G2_NS) g2_step<C + 1>(lbase, c, accA, accB, cur, prev);
}

__global__ __launch_bounds__(768, 3) void k_gemm2(const char* __restrict__ ws, const char* __restrict__ xin,
                                                  char* __restrict__ out) {
  __shared__ __attribute__((aligned(16))) char smem[G2_LDS];
  const char* oh = ws + WS_OH;
  const int tid = threadIdx.x, lane = tid & 63;
  const int wid = __builtin_amdgcn_readfirstlane(tid >> 6);
  const int b = blockIdx.x;
  const int x = b & 7, idx = b >> 3;
  const int rt = (x >> 1) * 8 + (idx >> 2), ct = (x & 1) * 4 + (idx & 3);
  const int row0 = rt * 64, col0 = ct * 80;
  const int kk = wid >> 2, mq = wid & 3;
  G2Ctx c;
  if (wid >= 8) {
    c.wid = wid - 8;
    const int r = lane >> 3, ch = (lane & 7) ^ r;
    c.a_src = oh + ((size_t)(row0 + c.wid * 16 + r) * KOH + ch * 8) * 2;
    c.b_src = ws + WS_P2 + (size_t)ct * 34 * 10240 + c.wid * 5120 + lane * 16;
    g2_issue<0>(smem, c); g2_issue<1>(smem, c); g2_issue<2>(smem, c);
    g2_load_step<0>(smem, c);
    __builtin_amdgcn_s_barrier(); __builtin_amdgcn_s_barrier();
    return;
  }
  c.wid = wid;
  const int g = lane >> 4, q = (lane >> 2) & 3, p = lane & 3, fr = lane & 15;
  c.a_rd = kk * 8192 + (mq * 16 + fr) * 128 + ((g ^ (lane & 7)) << 4);
  c.b_rd = 16384 + kk * 10240 + ((g >> 1) * 20 + (g & 1)) * 128 + q * 32 + p * 8;

  f32x4 accA[5], accB[5];
#pragma unroll
  for (int n = 0; n < 5; ++n) { accA[n] = f32x4{0.f, 0.f, 0.f, 0.f}; accB[n] = accA[n]; }
  const size_t go = ((size_t)(row0 + mq * 16 + fr) * D_ + col0 + g * 4) * 2;
  u32x2 xv[3];
#pragma unroll
  for (int n = 0; n < 3; ++n) xv[n] = *(const u32x2*)(xin + go + (kk ? (n < 2 ? n + 3 : 4) : n) * 32);
  {
    G2Frag f0_, f1_;
    g2_step<0>((unsigned)(size_t)smem, c, accA, accB, f0_, f1_);
  }
  __builtin_amdgcn_s_barrier();
  f32x4* ex = (f32x4*)smem + wid * 6 * 64 + lane;
  if (kk == 0) {
#pragma unroll
    for (int n = 0; n < 2; ++n) { ex[n * 64] = accA[n + 3]; ex[(3 + n) * 64] = accB[n + 3]; }
  } else {
#pragma unroll
    for (int n = 0; n < 3; ++n) { ex[n * 64] = accA[n]; ex[(3 + n) * 64] = accB[n]; }
  }
  __syncthreads();
  const f32x4* pe = (const f32x4*)smem + (wid ^ 4) * 6 * 64 + lane;
  auto fin = [&](f32x4 a, f32x4 bq, u32x2 xx, int n) {
    const unsigned x0 = xx[0], x1 = xx[1];
    const float xs[4] = {bflo(x0), bfhi(x0), bflo(x1), bfhi(x1)};
    float r[4];
#pragma unroll
    for (int e = 0; e < 4; ++e) r[e] = rbf(xs[e] + rbf(a[e])) + rbf(bq[e]);
    u32x2 o;
    o[0] = pack2(r[0], r[1]);
    o[1] = pack2(r[2], r[3]);
    *(u32x2*)(out + go + n * 32) = o;
  };
  if (kk == 0) {
#pragma unroll
    for (int n = 0; n < 3; ++n) fin(accA[n] + pe[n * 64], accB[n] + pe[(3 + n) * 64], xv[n], n);
  } else {
#pragma unroll
    for (int n = 0; n < 2; ++n) fin(pe[n * 64] + accA[n + 3], pe[(3 + n) * 64] + accB[n + 3], xv[n], n + 3);
  }
}

extern "C" void kernel_launch(void* const* d_in, const int* in_sizes, int n_in, void* d_out, int out_size,
                              void* d_ws, size_t ws_size, hipStream_t stream) {
  const char* x = (const char*)d_in[0];
  const char* wqkv = (const char*)d_in[1];
  const char* wo = (const char*)d_in[2];
  const char* w1 = (const char*)d_in[4];
  const char* w3 = (const char*)d_in[5];
  const char* w2 = (const char*)d_in[6];
  char* ws = (char*)d_ws;
  k_ln<<<S_ / LN_W, 64 * LN_W, 0, stream>>>((const unsigned*)x, (unsigned*)ws);
  k_gemm1<<<256, 512, 0, stream>>>(ws, wqkv, wo, w1, w3, w2, ws);
  k_gemm2<<<256, 768, 0, stream>>>(ws, x, (char*)d_out);
}
```

```cpp
#include <hip/hip_runtime.h>
#include <hip/hip_bf16.h>
#include <stdint.h>
#include <type_traits>

typedef __attribute__((ext_vector_type(8))) short bf16x8;
typedef __attribute__((ext_vector_type(4))) short bf16x4;
typedef __attribute__((ext_vector_type(4))) float f32x4;
typedef __attribute__((ext_vector_type(2))) float f32x2;
typedef __attribute__((ext_vector_type(2))) __bf16 bf2;
typedef __attribute__((ext_vector_type(2))) unsigned u32x2;
typedef __attribute__((ext_vector_type(4))) unsigned u32x4;

constexpr int S_ = 2048, D_ = 640, F_ = 1536;
constexpr int KOH = D_ + F_;
constexpr unsigned WS_OH = 4u << 20, WS_P2 = 24u << 20;

__device__ __forceinline__ float rbf(float f) { return (float)(__bf16)f; }
__device__ __forceinline__ unsigned pack2(float lo, float hi) {
  bf2 v; v[0] = (__bf16)lo; v[1] = (__bf16)hi;
  return __builtin_bit_cast(unsigned, v);
}
__device__ __forceinline__ float bflo(unsigned u) { return __builtin_bit_cast(float, u << 16); }
__device__ __forceinline__ float bfhi(unsigned u) { return __builtin_bit_cast(float, u & 0xffff0000u); }
__device__ __forceinline__ f32x2 rbf2(f32x2 v) {
  const unsigned u = pack2(v[0], v[1]);
  return f32x2{bflo(u), bfhi(u)};
}
__device__ __forceinline__ float dot2(unsigned a, unsigned b, float c) {
  return __builtin_amdgcn_fdot2_f32_bf16(__builtin_bit_cast(bf2, a), __builtin_bit_cast(bf2, b), c, false);
}
constexpr int ST_AUX = 16;
typedef __amdgpu_buffer_rsrc_t rsrc_t;
__device__ __forceinline__ rsrc_t mk_rsrc(void* p, unsigned bytes) {
  return __builtin_amdgcn_make_buffer_rsrc(p, 0, bytes, 0x00020000);
}
__device__ __forceinline__ void st16(rsrc_t r, unsigned off, u32x4 v) {
  __builtin_amdgcn_raw_buffer_store_b128(v, r, off, 0, ST_AUX);
}
__device__ __forceinline__ void st8(rsrc_t r, unsigned off, u32x2 v) {
  __builtin_amdgcn_raw_buffer_store_b64(v, r, off, 0, ST_AUX);
}
template <int N> __device__ __forceinline__ void wait_vm() {
  asm volatile("s_waitcnt vmcnt(%0)" ::"i"(N) : "memory");
}
__device__ __forceinline__ void glds16(const void* g, char* l) {
  __builtin_amdgcn_global_load_lds((const unsigned*)g, (unsigned*)l, 16, 0, 0);
}
__device__ __forceinline__ void glds16_lo(const void* g, unsigned lds_addr, unsigned long long mask) {
  unsigned keep;
  asm volatile(
      "s_mov_b32 %0, m0\n\ts_mov_b32 m0, %2\n\ts_mov_b64 exec, %3\n\tglobal_load_lds_dwordx4 %1, off\n\t"
      "s_mov_b64 exec, -1\n\ts_mov_b32 m0, %0"
      : "=&s"(keep) : "v"(g), "s"(lds_addr), "s"(mask) : "memory");
}
template <int IMM>
__device__ __forceinline__ void tr_issue(bf16x4& d, unsigned addr) {
  asm volatile("ds_read_b64_tr_b16 %0, %1 offset:%2" : "=v"(d) : "v"(addr), "i"(IMM) : "memory");
}
template <int IMM>
__device__ __forceinline__ void rd128_issue(bf16x8& d, unsigned addr) {
  asm volatile("ds_read_b128 %0, %1 offset:%2" : "=v"(d) : "v"(addr), "i"(IMM) : "memory");
}
__device__ __forceinline__ void pin(bf16x8 (&a)[4]) {
  asm volatile("" : "+v"(a[0]), "+v"(a[1]), "+v"(a[2]), "+v"(a[3]));
}
__device__ __forceinline__ void pin(bf16x8 (&a)[5]) {
  asm volatile("" : "+v"(a[0]), "+v"(a[1]), "+v"(a[2]), "+v"(a[3]), "+v"(a[4]));
}
__device__ __forceinline__ void lgkm_pin(bf16x4 (&l)[4], bf16x4 (&h)[4]) {
  asm volatile("s_waitcnt lgkmcnt(0)"
               : "+v"(l[0]), "+v"(l[1]), "+v"(l[2]), "+v"(l[3]), "+v"(h[0]), "+v"(h[1]), "+v"(h[2]), "+v"(h[3])
               :: "memory");
}
__device__ __forceinline__ void lgkm_pin(bf16x4 (&l)[5], bf16x4 (&h)[5]) {
  asm volatile("s_waitcnt lgkmcnt(0)"
               : "+v"(l[0]), "+v"(l[1]), "+v"(l[2]), "+v"(l[3]), "+v"(l[4]), "+v"(h[0]), "+v"(h[1]), "+v"(h[2]),
                 "+v"(h[3]), "+v"(h[4])
               :: "memory");
}
__device__ __forceinline__ void lgkm_pin(bf16x4 (&l)[6], bf16x4 (&h)[6]) {
  asm volatile("s_waitcnt lgkmcnt(0)"
               : "+v"(l[0]), "+v"(l[1]), "+v"(l[2]), "+v"(l[3]), "+v"(l[4]), "+v"(l[5]), "+v"(h[0]), "+v"(h[1]),
                 "+v"(h[2]), "+v"(h[3]), "+v"(h[4]), "+v"(h[5])
               :: "memory");
}
__device__ __forceinline__ bf16x8 cat8(bf16x4 a, bf16x4 b) {
  return __builtin_shufflevector(a, b, 0, 1, 2, 3, 4, 5, 6, 7);
}

constexpr int LN_W = 4;
constexpr int PF_BLOCKS = (D_ * 3 * D_ + D_ * D_ + 3 * D_ * F_) * 2 / 16 / 1024;
__global__ __launch_bounds__(64 * LN_W) void k_ln(const unsigned* __restrict__ x, unsigned* __restrict__ xn,
                                                  const char* __restrict__ wqkv, const char* __restrict__ wo,
                                                  const char* __restrict__ w1, const char* __restrict__ w3,
                                                  const char* __restrict__ w2) {
  if (blockIdx.x >= S_ / LN_W) {
    const int pb = blockIdx.x - S_ / LN_W;
    const char* src = pb < 150 ? wqkv + (size_t)pb * 16384
                    : pb < 200 ? wo + (size_t)(pb - 150) * 16384
                    : pb < 320 ? w1 + (size_t)(pb - 200) * 16384
                    : pb < 440 ? w3 + (size_t)(pb - 320) * 16384
                               : w2 + (size_t)(pb - 440) * 16384;
    u32x4 v[4];
#pragma unroll
    for (int i = 0; i < 4; ++i) v[i] = *(const u32x4*)(src + (i * 256 + threadIdx.x) * 16);
#pragma unroll
    for (int i = 0; i < 4; ++i) asm volatile("" ::"v"(v[i]));
    return;
  }
  const int lane = threadIdx.x & 63;
  const int row = blockIdx.x * LN_W + (threadIdx.x >> 6);
  const unsigned* xr = x + (size_t)row * (D_ / 2);
  unsigned v[5];
#pragma unroll
  for (int i = 0; i < 5; ++i) v[i] = xr[lane + 64 * i];
  float a[10];
  float s = 0.f;
#pragma unroll
  for (int i = 0; i < 5; ++i) {
    a[2 * i] = bflo(v[i]); a[2 * i + 1] = bfhi(v[i]);
    s += a[2 * i] + a[2 * i + 1];
  }
#pragma unroll
  for (int o = 32; o > 0; o >>= 1) s += __shfl_xor(s, o);
  const float mean = s * (1.0f / D_);
  float q = 0.f;
#pragma unroll
  for (int i = 0; i < 10; ++i) { float d = a[i] - mean; q += d * d; }
#pragma unroll
  for (int o = 32; o > 0; o >>= 1) q += __shfl_xor(q, o);
  const float var = q * (1.0f / D_);
  const float mb = rbf(mean);
  const float sd = rbf(sqrtf(rbf(rbf(var) + 1e-5f)));
  unsigned* o = xn + (size_t)row * (D_ / 2);
#pragma unroll
  for (int i = 0; i < 5; ++i) {
    float l = rbf(a[2 * i] - mb) / sd;
    float h = rbf(a[2 * i + 1] - mb) / sd;
    o[lane + 64 * i] = pack2(l, h);
  }
}

constexpr int N_P2 = 8 * 34 * 640;
__device__ __forceinline__ const char* p2_src(int i, const char* wo, const char* w2) {
  const int ct = i / (34 * 640), cc = (i / 640) % 34, j = i % 640;
  const int kk = j / 320, jj = j % 320;
  const int bi = jj >> 3, q = (jj >> 1) & 3, h = jj & 1;
  const int ghi = bi / 20, U = (bi % 20) >> 2, pos = bi & 3;
  const int k = 64 * cc + 32 * kk + 4 * (4 * ghi + 2 * (pos & 1) + (pos >> 1)) + q;
  return (k < D_ ? wo + (size_t)k * D_ * 2 : w2 + (size_t)(k - D_) * D_ * 2) + (80 * ct + 16 * U + 8 * h) * 2;
}
struct PackRegs { u32x4 v0, v1; };
__device__ __forceinline__ void pack_load(PackRegs& p, const char* wo, const char* w2) {
  const int i0 = blockIdx.x * 680 + threadIdx.x;
  p.v0 = *(const u32x4*)p2_src(i0, wo, w2);
  p.v1 = *(const u32x4*)p2_src(i0 + (threadIdx.x < 168 ? 512 : 0), wo, w2);
}
__device__ __forceinline__ void pack_store(const PackRegs& p, char* ws) {
  const unsigned i0 = blockIdx.x * 680 + threadIdx.x;
  *(u32x4*)(ws + WS_P2 + (size_t)i0 * 16) = p.v0;
  *(u32x4*)(ws + WS_P2 + (size_t)(i0 + (threadIdx.x < 168 ? 512 : 0)) * 16) = p.v1;
}

constexpr int G1_LDS = 147456;

struct GuCtx {
  const char* a_src;
  const char* b_src[3];
  int a_rd;
  int b_rd[6];
  int wid;
};

template <int T, int J>
__device__ __forceinline__ void gu_issue1(char* smem, const GuCtx& c) {
  constexpr int ST = (T + 4) / 2;
  constexpr int HF = (T + 4) & 1;
  constexpr int BT = T + 3;
  if constexpr (J == 0) {
    if constexpr (ST < 10)
      glds16(c.a_src + (size_t)HF * 64 * D_ * 2 + ST * 128, smem + (ST % 3) * 16384 + (HF * 64 + c.wid * 8) * 128);
  } else {
    if constexpr (BT >= 0 && BT < 20)
      glds16(c.b_src[J - 1] + (size_t)BT * 32 * F_ * 2, smem + 49152 + (BT & 3) * 24576 + (c.wid * 3 + J - 1) * 1024);
  }
}
template <int T>
__device__ __forceinline__ void gu_issue(char* smem, const GuCtx& c) {
  gu_issue1<T, 0>(smem, c); gu_issue1<T, 1>(smem, c); gu_issue1<T, 2>(smem, c); gu_issue1<T, 3>(smem, c);
}
template <int T> constexpr int gu_gsize() {
  return (T < -4) ? 0 : (((T + 4) / 2 < 10) ? 1 : 0) + ((T + 3 >= 0 && T + 3 < 20) ? 3 : 0);
}

struct GuFrag { bf16x8 a[4]; bf16x4 bl[6], bh[6]; };

template <int T, int I0>
__device__ __forceinline__ void gu_side(char* smem, unsigned ab, unsigned bb, const GuCtx& c, GuFrag& cur) {
  if constexpr (T < 20) {
    if constexpr (I0 % 6 == 3) gu_issue1<T, I0 / 6>(smem, c);
    else {
      constexpr int I = I0 - (I0 + 2) / 6;
      if constexpr (I < 4) rd128_issue<((T / 2) % 3) * 16384 + I * 2048>(cur.a[I], ab);
      else if constexpr (I < 16) {
        constexpr int n = (I - 4) >> 1;
        if constexpr (((I - 4) & 1) == 0) tr_issue<(T & 1) * 24576>(cur.bl[n], bb + c.b_rd[n]);
        else tr_issue<(T & 1) * 24576 + 3072>(cur.bh[n], bb + c.b_rd[n]);
      }
    }
  }
}
template <int T, int I>
__device__ __forceinline__ void gu_slots(char* smem, unsigned ab, unsigned bb, const GuCtx& c, f32x4 (&acc)[4][6],
                                         GuFrag& prev, GuFrag& cur) {
  if constexpr (T > 0) {
    constexpr int m = I / 6, n = I % 6;
    acc[m][n] = __builtin_amdgcn_mfma_f32_16x16x32_bf16(cat8(prev.bl[n], prev.bh[n]), prev.a[m], acc[m][n], 0, 0, 0);
  }
  gu_side<T, I>(smem, ab, bb, c, cur);
  __builtin_amdgcn_sched_barrier(0);
  if constexpr (I + 1 < 24) gu_slots<T, I + 1>(smem, ab, bb, c, acc, prev, cur);
}

template <int T>
__device__ __forceinline__ void gu_step(char* smem, unsigned lbase, const GuCtx& c, f32x4 (&acc)[4][6],
                                        GuFrag& prev, GuFrag& cur) {
  __builtin_amdgcn_sched_barrier(0);
  if constexpr (T > 0) { lgkm_pin(prev.bl, prev.bh); pin(prev.a); }
  if constexpr (T < 20) {
    wait_vm<gu_gsize<T - 2>() + gu_gsize<T - 1>()>();
    __builtin_amdgcn_s_barrier();
  }
  const unsigned bb = lbase + 49152 + ((T & 3) >> 1) * 49152;
  const unsigned ab = lbase + ((T & 1) ? (c.a_rd ^ 64) : c.a_rd);
  gu_slots<T, 0>(smem, ab, bb, c, acc, prev, cur);
  if constexpr (T < 20) gu_step<T + 1>(smem, lbase, c, acc, cur, prev);
}

__device__ __forceinline__ void gu_block(char* smem, int rt, int slice, const char* x, const char* w1,
                                         const char* w3, const char* wo, const char* w2, char* ws) {
  const int tid = threadIdx.x, lane = tid & 63;
  const int wid = __builtin_amdgcn_readfirstlane(tid >> 6);
  const int wm = wid >> 2, wn = wid & 3;
  const int row0 = rt * 128, f0 = slice * 192;
  char* oh = ws + WS_OH;
  PackRegs pk;
  pack_load(pk, wo, w2);
  GuCtx c;
  c.wid = wid;
  {
    const int r = lane >> 3, ch = (lane & 7) ^ r;
    c.a_src = x + ((size_t)(row0 + wid * 8 + r) * D_ + ch * 8) * 2;
  }
#pragma unroll
  for (int j = 0; j < 3; ++j) {
    const int off = (wid * 3 + j) * 1024 + lane * 16;
    const int k = off / 768, rem = off % 768;
    const int seg = rem >> 8, up = (rem >> 5) & 7, h = (rem >> 4) & 1;
    const int f = (k & 3) | (((k >> 3) & 1) << 2);
    const int col = (seg * 8 + (up ^ f)) * 16 + h * 8;
    const char* w = col < 192 ? w1 : w3;
    const int cc = col < 192 ? col : col - 192;
    c.b_src[j] = w + ((size_t)k * F_ + f0 + cc) * 2;
  }
  const int g = lane >> 4, q = (lane >> 2) & 3, p = lane & 3, fr = lane & 15;
  c.a_rd = (wm * 64 + fr) * 128 + ((g ^ (lane & 7)) << 4);
#pragma unroll
  for (int n = 0; n < 6; ++n) {
    const int U = (n < 3) ? 3 * wn + n : 12 + 3 * wn + (n - 3);
    const int f = q | ((g & 1) << 2);
    c.b_rd[n] = (8 * g + q) * 768 + (U >> 3) * 256 + (((U & 7) ^ f) << 5) + p * 8;
  }
  f32x4 acc[4][6];
#pragma unroll
  for (int m = 0; m < 4; ++m)
#pragma unroll
    for (int n = 0; n < 6; ++n) acc[m][n] = f32x4{0.f, 0.f, 0.f, 0.f};
  gu_issue<-4>(smem, c);
  gu_issue<-3>(smem, c);
  gu_issue<-2>(smem, c);
  gu_issue<-1>(smem, c);
  pack_store(pk, ws);
  {
    GuFrag f0_, f1_;
    gu_step<0>(smem, (unsigned)(size_t)smem, c, acc, f0_, f1_);
  }
  __builtin_amdgcn_s_barrier();
  const rsrc_t ro = mk_rsrc(oh, S_ * KOH * 2);
#pragma unroll
  for (int m = 0; m < 4; ++m) {
#pragma unroll
    for (int n = 0; n < 3; ++n) {
      u32x2 o;
#pragma unroll
      for (int r = 0; r < 2; ++r) {
        const f32x2 gt = rbf2(f32x2{acc[m][n][2 * r], acc[m][n][2 * r + 1]});
        const f32x2 up = rbf2(f32x2{acc[m][n + 3][2 * r], acc[m][n + 3][2 * r + 1]});
        const f32x2 t = gt * -1.4426950408889634f;
        f32x2 e = {__builtin_amdgcn_exp2f(t[0]), __builtin_amdgcn_exp2f(t[1])};
        e += 1.0f;
        const f32x2 rc = {__builtin_amdgcn_rcpf(e[0]), __builtin_amdgcn_rcpf(e[1])};
        const f32x2 hv = rbf2(gt * rc) * up;
        o[r] = pack2(hv[0], hv[1]);
      }
      *(u32x2*)(smem + (wm * 64 + m * 16 + fr) * 400 + (wn * 48 + n * 16 + g * 4) * 2) = o;
    }
  }
  __syncthreads();
#pragma unroll
  for (int i = 0; i < 6; ++i) {
    const int idx = tid + 512 * i;
    const int rr = idx / 24, ch = idx % 24;
    const u32x4 v = *(const u32x4*)(smem + rr * 400 + ch * 16);
    st16(ro, ((row0 + rr) * KOH + D_ + f0) * 2 + ch * 16, v);
  }
}

struct QkvCtx {
  const char* a_src;
  const char* ah_src;
  const char* ah_src1;
  const char* b_src[2];
  int a_rd;
  int b_rd[4];
  int wid;
};
constexpr int QA_STAGE = 144 * 128;
constexpr int QB_BASE = 3 * QA_STAGE;
constexpr int QB_SLOT = 32 * 512;

template <int T, int J>
__device__ __forceinline__ void qkv_issue1(char* smem, const QkvCtx& c) {
  constexpr int ST = (T + 4) / 2;
  constexpr int HF = (T + 4) & 1;
  constexpr int BT = T + 3;
  if constexpr (J == 0) {
    if constexpr (ST < 10)
      glds16(c.a_src + (size_t)HF * 64 * D_ * 2 + ST * 128,
             smem + (ST % 3) * QA_STAGE + (16 + HF * 64 + c.wid * 8) * 128);
  } else if constexpr (J == 1) {
    if constexpr (ST < 10)
      glds16_lo((HF ? c.ah_src1 : c.ah_src) + ST * 128,
                (unsigned)(size_t)smem + (ST % 3) * QA_STAGE + (HF * 8 + c.wid) * 128, 0xffull);
  } else {
    if constexpr (BT >= 0 && BT < 20)
      glds16(c.b_src[J - 2] + (size_t)BT * 32 * (3 * D_) * 2,
             smem + QB_BASE + (BT & 3) * QB_SLOT + (c.wid * 2 + J - 2) * 1024);
  }
}
template <int T>
__device__ __forceinline__ void qkv_issue(char* smem, const QkvCtx& c) {
  qkv_issue1<T, 0>(smem, c); qkv_issue1<T, 1>(smem, c); qkv_issue1<T, 2>(smem, c); qkv_issue1<T, 3>(smem, c);
}
template <int T> constexpr int qkv_gsize() {
  return (T < -4) ? 0 : (((T + 4) / 2 < 10) ? 2 : 0) + ((T + 3 >= 0 && T + 3 < 20) ? 2 : 0);
}

template <int MT> struct QkvFrag { bf16x8 a[MT]; bf16x4 bl[4], bh[4]; };

template <int T, int MT, int I0>
__device__ __forceinline__ void qkv_side(char* smem, unsigned ab, unsigned bb, const QkvCtx& c, QkvFrag<MT>& cur) {
  if constexpr (T < 20) {
    if constexpr (I0 % MT == 2) qkv_issue1<T, I0 / MT>(smem, c);
    else {
      constexpr int I = I0 - (I0 + MT - 3) / MT;
      if constexpr (I < MT) rd128_issue<((T / 2) % 3) * QA_STAGE + I * 2048>(cur.a[I], ab);
      else if constexpr (I < MT + 8) {
        constexpr int n = (I - MT) >> 1;
        if constexpr (((I - MT) & 1) == 0) tr_issue<(T & 1) * QB_SLOT>(cur.bl[n], bb + c.b_rd[n]);
        else tr_issue<(T & 1) * QB_SLOT + 2048>(cur.bh[n], bb + c.b_rd[n]);
      }
    }
  }
}
template <int T, int MT, int I>
__device__ __forceinline__ void qkv_slots(char* smem, unsigned ab, unsigned bb, const QkvCtx& c, f32x4 (&acc)[MT][4],
                                          QkvFrag<MT>& prev, QkvFrag<MT>& cur) {
  if constexpr (T > 0) {
    constexpr int m = I / 4, n = I % 4;
    acc[m][n] = __builtin_amdgcn_mfma_f32_16x16x32_bf16(cat8(prev.bl[n], prev.bh[n]), prev.a[m], acc[m][n], 0, 0, 0);
  }
  qkv_side<T, MT, I>(smem, ab, bb, c, cur);
  __builtin_amdgcn_sched_barrier(0);
  if constexpr (I + 1 < MT * 4) qkv_slots<T, MT, I + 1>(smem, ab, bb, c, acc, prev, cur);
}

template <int T, int MT>
__device__ __forceinline__ void qkv_step(char* smem, unsigned lbase, const QkvCtx& c, f32x4 (&acc)[MT][4],
                                         QkvFrag<MT>& prev, QkvFrag<MT>& cur) {
  __builtin_amdgcn_sched_barrier(0);
  if constexpr (T > 0) { lgkm_pin(prev.bl, prev.bh); pin(prev.a); }
  if constexpr (T < 20) {
    wait_vm<qkv_gsize<T - 2>() + qkv_gsize<T - 1>()>();
    __builtin_amdgcn_s_barrier();
  }
  const unsigned ab = lbase + ((T & 1) ? (c.a_rd ^ 64) : c.a_rd);
  const unsigned bb = lbase + QB_BASE + ((T & 3) >> 1) * 2 * QB_SLOT;
  qkv_slots<T, MT, 0>(smem, ab, bb, c, acc, prev, cur);
  if constexpr (T < 20) qkv_step<T + 1, MT>(smem, lbase, c, acc, cur, prev);
}

constexpr int QE_STRIDE = 488;

template <int MT>
__device__ __forceinline__ void qkv_main(char* smem, const QkvCtx& c, int lane, int arow0, int wn) {
  f32x4 acc[MT][4];
#pragma unroll
  for (int m = 0; m < MT; ++m)
#pragma unroll
    for (int n = 0; n < 4; ++n) acc[m][n] = f32x4{0.f, 0.f, 0.f, 0.f};
  {
    QkvFrag<MT> f0_, f1_;
    qkv_step<0, MT>(smem, (unsigned)(size_t)smem, c, acc, f0_, f1_);
  }
  __builtin_amdgcn_s_barrier();
  const int g = lane >> 4, fr = lane & 15;
#pragma unroll
  for (int n = 0; n < 4; ++n) {
    const int U = wn * 4 + n;
    if (U < 15) {
#pragma unroll
      for (int m = 0; m < MT; ++m) {
        u32x2 o;
        o[0] = pack2(acc[m][n][0], acc[m][n][1]);
        o[1] = pack2(acc[m][n][2], acc[m][n][3]);
        *(u32x2*)(smem + (arow0 + m * 16 + fr) * QE_STRIDE + U * 32 + g * 8) = o;
      }
    }
  }
}

__device__ __forceinline__ void qkv_block(char* smem, int rt, int hg, const char* x, const char* wqkv,
                                          const char* wo, const char* w2, char* ws) {
  const int tid = threadIdx.x, lane = tid & 63;
  const int wid = __builtin_amdgcn_readfirstlane(tid >> 6);
  const int wm = wid >> 2, wn = wid & 3;
  const int s0 = rt * 128;
  char* oh = ws + WS_OH;
  PackRegs pk;
  pack_load(pk, wo, w2);
  QkvCtx c;
  c.wid = wid;
  {
    const int r = lane >> 3, ch = (lane & 7) ^ r;
    c.a_src = x + ((size_t)(s0 + wid * 8 + r) * D_ + ch * 8) * 2;
    const int chh = (lane & 7) ^ wid;
    int r0 = s0 - 16 + wid, r1 = s0 - 8 + wid;
    r0 = r0 < 0 ? 0 : r0;
    r1 = r1 < 0 ? 0 : r1;
    c.ah_src = x + ((size_t)r0 * D_ + chh * 8) * 2;
    c.ah_src1 = x + ((size_t)r1 * D_ + chh * 8) * 2;
  }
#pragma unroll
  for (int j = 0; j < 2; ++j) {
    const int off = (wid * 2 + j) * 1024 + lane * 16;
    const int k = off >> 9, rem = off & 511;
    const int seg = rem >> 8, up = (rem >> 5) & 7, h = (rem >> 4) & 1;
    const int f = (k & 3) | (((k >> 3) & 1) << 2);
    int U = seg * 8 + (up ^ f);
    U = U > 14 ? 14 : U;
    const int sec = U / 5, uu = U % 5;
    c.b_src[j] = wqkv + ((size_t)k * (3 * D_) + sec * D_ + hg * 80 + uu * 16 + h * 8) * 2;
  }
  const int g = lane >> 4, q = (lane >> 2) & 3, p = lane & 3, fr = lane & 15;
  const int arow0 = wm ? 80 : 0;
  c.a_rd = (arow0 + fr) * 128 + ((g ^ (lane & 7)) << 4);
#pragma unroll
  for (int n = 0; n < 4; ++n) {
    const int U = wn * 4 + n;
    const int f = q | ((g & 1) << 2);
    c.b_rd[n] = (8 * g + q) * 512 + (U >> 3) * 256 + (((U & 7) ^ f) << 5) + p * 8;
  }
  qkv_issue<-4>(smem, c);
  qkv_issue<-3>(smem, c);
  qkv_issue<-2>(smem, c);
  qkv_issue<-1>(smem, c);
  pack_store(pk, ws);
  if (wm == 0) qkv_main<5>(smem, c, lane, arow0, wn);
  else qkv_main<4>(smem, c, lane, arow0, wn);
  __syncthreads();

  const int hh = tid >> 7, r = tid & 127;
  const int s = s0 + r;
  const char* qp = smem + (16 + r) * QE_STRIDE + hh * 40;
  unsigned qv[10];
#pragma unroll
  for (int i = 0; i < 5; ++i) {
    u32x2 t = *(const u32x2*)(qp + i * 8);
    qv[2 * i] = t[0]; qv[2 * i + 1] = t[1];
  }
  float lg[8];
  float mx = -3.0e38f;
#pragma unroll
  for (int w = 0; w < 8; ++w) {
    const char* kp = qp + 160 - 2 * w * QE_STRIDE;
    float d = 0.f;
#pragma unroll
    for (int i = 0; i < 5; ++i) {
      u32x2 t = *(const u32x2*)(kp + i * 8);
      d = dot2(qv[2 * i], t[0], d);
      d = dot2(qv[2 * i + 1], t[1], d);
    }
    float l = rbf(rbf(d) * 0.22360679774997896f);
    l = (s - 2 * w >= 0) ? l : rbf(-1e9f);
    lg[w] = l;
    mx = fmaxf(mx, l);
  }
  float sum = 0.f;
#pragma unroll
  for (int w = 0; w < 8; ++w) { lg[w] = __expf(lg[w] - mx); sum += lg[w]; }
  const float inv = 1.0f / sum;
  f32x2 o[10];
#pragma unroll
  for (int i = 0; i < 10; ++i) o[i] = f32x2{0.f, 0.f};
#pragma unroll
  for (int w = 0; w < 8; ++w) {
    const float pw = rbf(lg[w] * inv);
    const char* vp = qp + 320 - 2 * w * QE_STRIDE;
#pragma unroll
    for (int i = 0; i < 5; ++i) {
      u32x2 t = *(const u32x2*)(vp + i * 8);
      const unsigned t0 = t[0], t1 = t[1];
      o[2 * i] += pw * f32x2{bflo(t0), bfhi(t0)};
      o[2 * i + 1] += pw * f32x2{bflo(t1), bfhi(t1)};
    }
  }
#pragma unroll
  for (int i = 0; i < 5; ++i) {
    u32x2 t;
    t[0] = pack2(o[2 * i][0], o[2 * i][1]);
    t[1] = pack2(o[2 * i + 1][0], o[2 * i + 1][1]);
    *(u32x2*)(smem + (16 + r) * QE_STRIDE + hh * 40 + i * 8) = t;
  }
  __syncthreads();
  const rsrc_t ro = mk_rsrc(oh, S_ * KOH * 2);
#pragma unroll
  for (int i = 0; i < 5; ++i) {
    const int idx = tid + 512 * i;
    const int rr = idx / 20, part = idx % 20;
    u32x2 t = *(const u32x2*)(smem + (16 + rr) * QE_STRIDE + part * 8);
    st8(ro, ((s0 + rr) * KOH + hg * 80) * 2 + part * 8, t);
  }
}

__global__ __launch_bounds__(512, 2) void k_gemm1(const char* x, const char* __restrict__ wqkv,
                                                  const char* __restrict__ wo, const char* __restrict__ w1,
                                                  const char* __restrict__ w3, const char* __restrict__ w2,
                                                  char* ws) {
  __shared__ __attribute__((aligned(16))) char smem[G1_LDS];
  const int b = blockIdx.x;
  const int xc = b & 7, idx = b >> 3;
  const int rh = xc & 1, j = xc >> 1;
  const int i2 = idx & 15;
  const int rt = rh * 8 + (i2 >> 1), cs = 2 * j + (i2 & 1);
  if (idx < 16) gu_block(smem, rt, cs, x, w1, w3, wo, w2, ws);
  else qkv_block(smem, rt, cs, x, wqkv, wo, w2, ws);
}

constexpr int G2_STAGE = 16384 + 20480;
constexpr int G2_LDS = 4 * G2_STAGE;
constexpr int G2_NS = KOH / 128;

struct G2Ctx {
  const char* a_src;
  const char* b_src;
  int a_rd, b_rd;
  int wid;
};
struct G2Frag { bf16x8 a[2]; bf16x4 bl[5], bh[5], cl[5], ch[5]; };

template <int C>
__device__ __forceinline__ void g2_issue(char* smem, const G2Ctx& c) {
  if constexpr (C < G2_NS) {
    char* dst = smem + (C & 3) * G2_STAGE;
#pragma unroll
    for (int j = 0; j < 4; ++j)
      glds16(c.a_src + (size_t)(j & 1) * 8 * KOH * 2 + C * 256 + (j >> 1) * 128,
             dst + (j >> 1) * 8192 + (c.wid * 2 + (j & 1)) * 1024);
#pragma unroll
    for (int j = 0; j < 5; ++j) glds16(c.b_src + C * 20480 + j * 1024, dst + 16384 + c.wid * 5120 + j * 1024);
  }
}
template <int C>
__device__ __forceinline__ void g2_load_step(char* smem, const G2Ctx& c) {
  constexpr int ahead = (G2_NS - 1 - C) < 2 ? (G2_NS - 1 - C) : 2;
  wait_vm<9 * ahead>();
  __builtin_amdgcn_s_barrier();
  g2_issue<C + 3>(smem, c);
  if constexpr (C + 1 < G2_NS) g2_load_step<C + 1>(smem, c);
}
template <int C>
__device__ __forceinline__ void g2_mma(f32x4 (&accA)[5], f32x4 (&accB)[5], G2Frag& prev) {
#pragma unroll
  for (int n = 0; n < 5; ++n) {
    if constexpr (C < 5)
      accA[n] = __builtin_amdgcn_mfma_f32_16x16x32_bf16(cat8(prev.bl[n], prev.bh[n]), prev.a[0], accA[n], 0, 0, 0);
    else
      accB[n] = __builtin_amdgcn_mfma_f32_16x16x32_bf16(cat8(prev.bl[n], prev.bh[n]), prev.a[0], accB[n], 0, 0, 0);
  }
#pragma unroll
  for (int n = 0; n < 5; ++n) {
    if constexpr (C < 5)
      accA[n] = __builtin_amdgcn_mfma_f32_16x16x32_bf16(cat8(prev.cl[n], prev.ch[n]), prev.a[1], accA[n], 0, 0, 0);
    else
      accB[n] = __builtin_amdgcn_mfma_f32_16x16x32_bf16(cat8(prev.cl[n], prev.ch[n]), prev.a[1], accB[n], 0, 0, 0);
  }
}
template <int C>
__device__ __forceinline__ void g2_step(unsigned lbase, const G2Ctx& c, f32x4 (&accA)[5], f32x4 (&accB)[5],
                                        G2Frag& prev, G2Frag& cur) {
  __builtin_amdgcn_sched_barrier(0);
  if constexpr (C > 0) {
    lgkm_pin(prev.bl, prev.bh);
    lgkm_pin(prev.cl, prev.ch);
    asm volatile("" : "+v"(prev.a[0]), "+v"(prev.a[1]));
  }
  if constexpr (C < G2_NS) {
    __builtin_amdgcn_s_barrier();
    const unsigned sb = lbase + (C & 3) * G2_STAGE;
    rd128_issue<0>(cur.a[0], sb + c.a_rd);
    rd128_issue<0>(cur.a[1], sb + (c.a_rd ^ 64));
    const unsigned ba = sb + c.b_rd;
    tr_issue<0>(cur.bl[0], ba);    tr_issue<256>(cur.bh[0], ba);
    tr_issue<512>(cur.bl[1], ba);  tr_issue<512 + 256>(cur.bh[1], ba);
    tr_issue<1024>(cur.bl[2], ba); tr_issue<1024 + 256>(cur.bh[2], ba);
    tr_issue<1536>(cur.bl[3], ba); tr_issue<1536 + 256>(cur.bh[3], ba);
    tr_issue<2048>(cur.bl[4], ba); tr_issue<2048 + 256>(cur.bh[4], ba);
    tr_issue<5120>(cur.cl[0], ba);        tr_issue<5120 + 256>(cur.ch[0], ba);
    tr_issue<5120 + 512>(cur.cl[1], ba);  tr_issue<5120 + 512 + 256>(cur.ch[1], ba);
    tr_issue<5120 + 1024>(cur.cl[2], ba); tr_issue<5120 + 1024 + 256>(cur.ch[2], ba);
    tr_issue<5120 + 1536>(cur.cl[3], ba); tr_issue<5120 + 1536 + 256>(cur.ch[3], ba);
    tr_issue<5120 + 2048>(cur.cl[4], ba); tr_issue<5120 + 2048 + 256>(cur.ch[4], ba);
    __builtin_amdgcn_sched_barrier(0);
  }
  if constexpr (C > 0) g2_mma<C - 1>(accA, accB, prev);
  if constexpr (C < G2_NS) g2_step<C + 1>(lbase, c, accA, accB, cur, prev);
}

__global__ __launch_bounds__(768, 3) void k_gemm2(const char* __restrict__ ws, const char* __restrict__ xin,
                                                  char* __restrict__ out) {
  __shared__ __attribute__((aligned(16))) char smem[G2_LDS];
  const char* oh = ws + WS_OH;
  const int tid = threadIdx.x, lane = tid & 63;
  const int wid = __builtin_amdgcn_readfirstlane(tid >> 6);
  const int b = blockIdx.x;
  const int x = b & 7, idx = b >> 3;
  const int rt = (x >> 1) * 8 + (idx >> 2), ct = (x & 1) * 4 + (idx & 3);
  const int row0 = rt * 64, col0 = ct * 80;
  const int kk = wid >> 2, mq = wid & 3;
  G2Ctx c;
  if (wid >= 8) {
    c.wid = wid - 8;
    const int r = lane >> 3, ch = (lane & 7) ^ r;
    c.a_src = oh + ((size_t)(row0 + c.wid * 16 + r) * KOH + ch * 8) * 2;
    c.b_src = ws + WS_P2 + (size_t)ct * 34 * 10240 + c.wid * 5120 + lane * 16;
    g2_issue<0>(smem, c); g2_issue<1>(smem, c); g2_issue<2>(smem, c);
    g2_load_step<0>(smem, c);
    __builtin_amdgcn_s_barrier(); __builtin_amdgcn_s_barrier();
    return;
  }
  c.wid = wid;
  const int g = lane >> 4, q = (lane >> 2) & 3, p = lane & 3, fr = lane & 15;
  c.a_rd = kk * 8192 + (mq * 16 + fr) * 128 + ((g ^ (lane & 7)) << 4);
  c.b_rd = 16384 + kk * 10240 + ((g >> 1) * 20 + (g & 1)) * 128 + q * 32 + p * 8;

  f32x4 accA[5], accB[5];
#pragma unroll
  for (int n = 0; n < 5; ++n) { accA[n] = f32x4{0.f, 0.f, 0.f, 0.f}; accB[n] = accA[n]; }
  const size_t go = ((size_t)(row0 + mq * 16 + fr) * D_ + col0 + g * 4) * 2;
  u32x2 xv[3];
#pragma unroll
  for (int n = 0; n < 3; ++n) xv[n] = *(const u32x2*)(xin + go + (kk ? (n < 2 ? n + 3 : 4) : n) * 32);
  {
    G2Frag f0_, f1_;
    g2_step<0>((unsigned)(size_t)smem, c, accA, accB, f0_, f1_);
  }
  __builtin_amdgcn_s_barrier();
  f32x4* ex = (f32x4*)smem + wid * 6 * 64 + lane;
  if (kk == 0) {
#pragma unroll
    for (int n = 0; n < 2; ++n) { ex[n * 64] = accA[n + 3]; ex[(3 + n) * 64] = accB[n + 3]; }
  } else {
#pragma unroll
    for (int n = 0; n < 3; ++n) { ex[n * 64] = accA[n]; ex[(3 + n) * 64] = accB[n]; }
  }
  __syncthreads();
  const f32x4* pe = (const f32x4*)smem + (wid ^ 4) * 6 * 64 + lane;
  auto fin = [&](f32x4 a, f32x4 bq, u32x2 xx, int n) {
    const unsigned x0 = xx[0], x1 = xx[1];
    const float xs[4] = {bflo(x0), bfhi(x0), bflo(x1), bfhi(x1)};
    float r[4];
#pragma unroll
    for (int e = 0; e < 4; ++e) r[e] = rbf(xs[e] + rbf(a[e])) + rbf(bq[e]);
    u32x2 o;
    o[0] = pack2(r[0], r[1]);
    o[1] = pack2(r[2], r[3]);
    *(u32x2*)(out + go + n * 32) = o;
  };
  if (kk == 0) {
#pragma unroll
    for (int n = 0; n < 3; ++n) fin(accA[n] + pe[n * 64], accB[n] + pe[(3 + n) * 64], xv[n], n);
  } else {
#pragma unroll
    for (int n = 0; n < 2; ++n) fin(pe[n * 64] + accA[n + 3], pe[(3 + n) * 64] + accB[n + 3], xv[n], n + 3);
  }
}

extern "C" void kernel_launch(void* const* d_in, const int* in_sizes, int n_in, void* d_out, int out_size,
                              void* d_ws, size_t ws_size, hipStream_t stream) {
  const char* x = (const char*)d_in[0];
  const char* wqkv = (const char*)d_in[1];
  const char* wo = (const char*)d_in[2];
  const char* w1 = (const char*)d_in[4];
  const char* w3 = (const char*)d_in[5];
  const char* w2 = (const char*)d_in[6];
  char* ws = (char*)d_ws;
  k_ln<<<S_ / LN_W + PF_BLOCKS, 64 * LN_W, 0, stream>>>((const unsigned*)x, (unsigned*)ws, wqkv, wo, w1, w3, w2);
  k_gemm1<<<256, 512, 0, stream>>>(ws, wqkv, wo, w1, w3, w2, ws);
  k_gemm2<<<256, 768, 0, stream>>>(ws, x, (char*)d_out);
}
```
